```python
import torch

def _softmax(x, axis=-1):
    return torch.softmax(x, dim=axis)
def _gelu(x):
    return torch.nn.functional.gelu(x, approximate="tanh")
def _silu(x):
    return torch.nn.functional.silu(x)
def _sigmoid(x):
    return torch.sigmoid(x)
def _layernorm(x, axis=-1, eps=1e-5):
    m = x.mean(dim=axis, keepdim=True)
    v = x.var(dim=axis, keepdim=True, unbiased=False)
    return (x - m) / torch.sqrt(v + eps)
def _scatter_add(tgt, idx, src, axis=0):
    return tgt.clone().index_add_(axis, idx, src)
def _segment_sum(src, seg, n):
    out = torch.zeros((n,) + src.shape[1:], dtype=src.dtype)
    return out.index_add_(0, seg, src)
def _pad(x, pw):
    flat = tuple(v for pair in reversed(pw) for v in pair)
    return torch.nn.functional.pad(x, flat)

kernel_name = 'concat_heads__linear_attn_block__ssm_conv_s4d__prompt'

def setup_inputs():

    _w = (lambda a: (lambda t: t.to(torch.bfloat16) if t.is_floating_point() else t)(torch.as_tensor(a)))
    def _A_setup():
        B, S, D = 1, 2048, 1024
        return {
            "x": _w((lambda *s: torch.randn(*s, dtype=torch.bfloat16) * 0.05)(B, S, D)),
            "wqkv": _w((lambda *s: torch.randn(*s, dtype=torch.bfloat16) * 0.8)(D, 3 * D)),
            "wo": _w((lambda *s: torch.randn(*s, dtype=torch.bfloat16) * 0.05)(D, D)),
        }
    _A_in = _A_setup()

    def _B_setup():
        Bsz, L, D, N = 1, 2048, 1024, 128
        return {
            "u": _w((lambda *s: torch.randn(*s, dtype=torch.bfloat16) * 0.05)(Bsz, L, D)),
            "log_A_real": _w((lambda *s: torch.randn(*s, dtype=torch.bfloat16) * 3.2)(D, N) * 0.5),
            "A_imag": _w((lambda *s: torch.randn(*s, dtype=torch.bfloat16) * 0.05)(D, N) * 3.14),
            "C_real": _w((lambda *s: torch.randn(*s, dtype=torch.bfloat16) * 0.8)(D, N) / N ** 0.5),
            "C_imag": _w((lambda *s: torch.randn(*s, dtype=torch.bfloat16) * 0.8)(D, N) / N ** 0.5),
            "log_dt": _w((lambda *s: torch.randn(*s, dtype=torch.bfloat16) * 0.05)(D) * 0.5 - 4.0),
            "D_skip": _w((lambda *s: torch.randn(*s, dtype=torch.bfloat16) * 0.1)(D)),
        }
    _B_in = _B_setup()

    return {
        "A_x": _A_in["x"],
        "A_wqkv": _A_in["wqkv"],
        "A_wo": _A_in["wo"],
        "B_u": _B_in["u"],
        "B_log_A_real": _B_in["log_A_real"],
        "B_A_imag": _B_in["A_imag"],
        "B_C_real": _B_in["C_real"],
        "B_C_imag": _B_in["C_imag"],
        "B_log_dt": _B_in["log_dt"],
        "B_D_skip": _B_in["D_skip"],
    }

def reference(A_x, A_wqkv, A_wo, B_u, B_log_A_real, B_A_imag, B_C_real, B_C_imag, B_log_dt, B_D_skip):
    def _A_ref(x, wqkv, wo):
        B, S, D = x.shape
        H = 32; Dh = D // H
        qkv = (lambda x, *d: x.permute(*d))((x @ wqkv).reshape(B, S, 3, H, Dh), 2, 0, 3, 1, 4)
        q, k, v = qkv[0], qkv[1], qkv[2]
        phi = lambda t: torch.where(t > 0, t + 1, torch.exp(t)) + 1e-6
        pq, pk = phi(q), phi(k)
        kv = torch.einsum("bhsd,bhse->bhde", pk, v)
        z = torch.einsum("bhsd->bhd", pk)
        num = torch.einsum("bhqd,bhde->bhqe", pq, kv)
        den = torch.einsum("bhqd,bhd->bhq", pq, z)[..., None] + 1e-6
        o = num / den
        return (lambda x, *d: x.permute(*d))(o, 0, 2, 1, 3).reshape(B, S, D) @ wo

    def _B_ref(u, log_A_real, A_imag, C_real, C_imag, log_dt, D_skip):
        L = u.shape[1]
        dt = torch.exp(log_dt)
        A = -torch.exp(log_A_real) + 1j * A_imag
        C = (C_real + 1j * C_imag) * dt[:, None]
        pos = (lambda x, dt: x.to(dt))(torch.arange(L), log_dt.dtype)
        P = torch.exp((A * dt[:, None])[:, :, None] * pos[None, None, :])
        K = 2.0 * torch.einsum("dn,dnl->dl", C, P).real
        L2 = 2 * L
        u_t = (lambda x, *d: x.permute(*d))(u, 0, 2, 1)
        Kf = (lambda x, *a, **k: torch.fft.rfft(x.float(), *a, **k))(K, L2)
        Uf = (lambda x, *a, **k: torch.fft.rfft(x.float(), *a, **k))(u_t, L2)
        y = (lambda x, *a, **k: torch.fft.irfft(x, *a, **k).to(torch.bfloat16))(Uf * Kf[None, :, :], L2)[:, :, :L]
        y = (lambda x, *d: x.permute(*d))(y, 0, 2, 1)
        return y + u * D_skip[None, None, :]

    a = _A_ref(A_x, A_wqkv, A_wo)
    b = _B_ref(B_u, B_log_A_real, B_A_imag, B_C_real, B_C_imag, B_log_dt, B_D_skip)
    return torch.concatenate([a, b], axis=-1)
```

```cpp
#include <hip/hip_runtime.h>
#include <hip/hip_bf16.h>
#include <stdint.h>

typedef unsigned short u16;
typedef __attribute__((ext_vector_type(8))) __bf16 bf16x8;
typedef __attribute__((ext_vector_type(2))) __bf16 bf16x2;
typedef __attribute__((ext_vector_type(4))) short s16x4;
typedef __attribute__((ext_vector_type(8))) short s16x8;
typedef __attribute__((ext_vector_type(16))) float f32x16;
typedef __attribute__((ext_vector_type(4))) float f32x4;
typedef __attribute__((ext_vector_type(2))) float f32x2;
typedef __attribute__((ext_vector_type(4))) unsigned u32x4;
typedef __attribute__((ext_vector_type(2))) unsigned u32x2;
#define LAS __attribute__((address_space(3)))
#define DI __device__ __forceinline__

#define MFMA32(a, b, c) __builtin_amdgcn_mfma_f32_32x32x16_bf16((a), (b), (c), 0, 0, 0)
#define MFMA16(a, b, c) __builtin_amdgcn_mfma_f32_16x16x32_bf16((a), (b), (c), 0, 0, 0)
#define BARRIER() asm volatile("s_waitcnt lgkmcnt(0)\n\ts_barrier" ::: "memory")
#define WAIT_VM(n) asm volatile("s_waitcnt vmcnt(" #n ")" ::: "memory")

DI unsigned pack2(float a, float b) {
  f32x2 v = {a, b};
  return __builtin_bit_cast(unsigned, __builtin_convertvector(v, bf16x2));
}
DI float lo2f(unsigned p) { return __builtin_bit_cast(float, p << 16); }
DI float hi2f(unsigned p) { return __builtin_bit_cast(float, p & 0xffff0000u); }
DI float b2f(u16 b) { return __builtin_bit_cast(float, (unsigned)b << 16); }
DI float rbf(float x) { return lo2f(pack2(x, x)); }
DI unsigned phi2(unsigned p) {
  float a = lo2f(p), b = hi2f(p);
  float ea = __builtin_amdgcn_exp2f(fminf(a, 0.f) * 1.44269504f) + fmaxf(a, 0.f);
  float eb = __builtin_amdgcn_exp2f(fminf(b, 0.f) * 1.44269504f) + fmaxf(b, 0.f);
  unsigned r = pack2(ea, eb);
  return pack2(lo2f(r) + 1e-6f, hi2f(r) + 1e-6f);
}
DI bf16x8 mk8(unsigned a, unsigned b, unsigned c, unsigned d) {
  u32x4 v = {a, b, c, d};
  return __builtin_bit_cast(bf16x8, v);
}
DI bf16x8 trfrag(const char* base, int off0, int off1) {
  s16x4 lo = __builtin_amdgcn_ds_read_tr16_b64_v4i16((LAS s16x4*)(base + off0));
  s16x4 hi = __builtin_amdgcn_ds_read_tr16_b64_v4i16((LAS s16x4*)(base + off1));
  s16x8 v = __builtin_shufflevector(lo, hi, 0, 1, 2, 3, 4, 5, 6, 7);
  return __builtin_bit_cast(bf16x8, v);
}

constexpr size_t WS_Q = 0;
constexpr size_t WS_O = 4u << 20;
constexpr size_t WS_KVP = 8u << 20;
constexpr size_t WS_ZP = 10u << 20;

constexpr int K1_ASTG = 16384, K1_BSTG = 24576;
constexpr int K1_BOFF = 3 * K1_ASTG;
constexpr int K1_LDS = 3 * (K1_ASTG + K1_BSTG);

__global__ __launch_bounds__(512, 2) void k1_qkv(const u16* __restrict__ x, const u16* __restrict__ w,
                                                 char* __restrict__ ws) {
  extern __shared__ __attribute__((aligned(16))) char smem[];
  const int tid = threadIdx.x, lane = tid & 63;
  const int wid = __builtin_amdgcn_readfirstlane(tid >> 6);
  const int bid = blockIdx.x;
  const int xcd = bid & 7, idx = bid >> 3;
  const int rb = (xcd & 1) * 8 + (idx & 7);
  const int cb = (xcd >> 1) * 4 + (idx >> 3);

  if (wid >= 4) {
    const int lw = wid - 4;
    const int r8 = lane >> 3, cp = lane & 7;
    unsigned aoff[4], boff[6];
#pragma unroll
    for (int ii = 0; ii < 4; ii++) {
      int row = (4 * lw + ii) * 8 + r8;
      int c = cp ^ ((row >> 1) & 7);
      aoff[ii] = (unsigned)((rb * 128 + row) * 2048 + c * 16);
    }
#pragma unroll
    for (int jj = 0; jj < 6; jj++) {
      int id = 6 * lw + jj;
      int sg = id >> 3, j = id & 7;
      int k = j * 8 + r8;
      int c = cp ^ (((k >> 1) & 1) << 2);
      boff[jj] = (unsigned)(k * 6144 + (sg * 1024 + cb * 64) * 2 + c * 16);
    }
    const char* xa = (const char*)x;
    const char* wb = (const char*)w;
    auto issue = [&](int kt) {
      int st = kt % 3;
      const char* ap = xa + kt * 128;
      const char* bp = wb + (size_t)kt * 64 * 6144;
      char* la = smem + st * K1_ASTG + lw * 4096;
      char* lb = smem + K1_BOFF + st * K1_BSTG + lw * 6144;
#pragma unroll
      for (int ii = 0; ii < 4; ii++)
        __builtin_amdgcn_global_load_lds((const unsigned*)(ap + aoff[ii]), (LAS unsigned*)(la + ii * 1024), 16, 0, 0);
#pragma unroll
      for (int jj = 0; jj < 6; jj++)
        __builtin_amdgcn_global_load_lds((const unsigned*)(bp + boff[jj]), (LAS unsigned*)(lb + jj * 1024), 16, 0, 0);
    };
    issue(0); issue(1); issue(2);
    WAIT_VM(20);
    BARRIER();
#pragma unroll
    for (int kt = 0; kt < 15; kt++) {
      if (kt + 2 < 16) WAIT_VM(10); else WAIT_VM(0);
      BARRIER();
      if (kt + 3 < 16) issue(kt + 3);
    }
    BARRIER();
    return;
  }
  const int wm = wid & 1, wn = wid >> 1;
  const int r = lane & 31, hh = lane >> 5;
  int aaddr[4];
#pragma unroll
  for (int ks = 0; ks < 4; ks++) aaddr[ks] = (wm * 64 + r) * 128 + (((2 * ks + hh) ^ ((r >> 1) & 7)) * 16);
  const int blk = (lane >> 4) & 1, q4 = (lane & 15) >> 2, p4 = lane & 3;
  const int baddr = K1_BOFF + (8 * hh + q4) * 128 + ((wn ^ ((q4 >> 1) & 1)) * 64) + blk * 32 + p4 * 8;

  f32x16 accq[2], acck[2], accv[2];
#pragma unroll
  for (int i = 0; i < 16; i++) { accq[0][i] = 0; accq[1][i] = 0; acck[0][i] = 0; acck[1][i] = 0; accv[0][i] = 0; accv[1][i] = 0; }
  bf16x8 fa[2][2], fb[2][3];
  auto rd = [&](int s, int buf) {
    int kt = s >> 2, ks = s & 3, st = kt % 3;
    const char* ab = smem + st * K1_ASTG + aaddr[ks];
    fa[buf][0] = *(const bf16x8*)(ab);
    fa[buf][1] = *(const bf16x8*)(ab + 4096);
    const char* bb = smem + baddr + st * K1_BSTG + ks * 2048;
#pragma unroll
    for (int sg = 0; sg < 3; sg++) fb[buf][sg] = trfrag(bb, sg * 8192, sg * 8192 + 512);
  };
  BARRIER();
  rd(0, 0);
#pragma unroll
  for (int s = 0; s < 64; s++) {
    const int cur = s & 1;
    if ((s & 3) == 3 && s < 63) BARRIER();
    if (s + 1 < 64) rd(s + 1, cur ^ 1);
    accq[0] = MFMA32(fb[cur][0], fa[cur][0], accq[0]);
    acck[0] = MFMA32(fa[cur][0], fb[cur][1], acck[0]);
    accv[0] = MFMA32(fa[cur][0], fb[cur][2], accv[0]);
    accq[1] = MFMA32(fb[cur][0], fa[cur][1], accq[1]);
    acck[1] = MFMA32(fa[cur][1], fb[cur][1], acck[1]);
    accv[1] = MFMA32(fa[cur][1], fb[cur][2], accv[1]);
  }
  const int head = cb * 2 + wn;
  f32x16 kv, zz;
#pragma unroll
  for (int i = 0; i < 16; i++) { kv[i] = 0; zz[i] = 0; }
  const bf16x8 ones = mk8(0x3f803f80u, 0x3f803f80u, 0x3f803f80u, 0x3f803f80u);
#pragma unroll
  for (int mt = 0; mt < 2; mt++) {
    const int rt = rb * 4 + wm * 2 + mt;
    char* qp = ws + WS_Q + ((size_t)(rt * 32 + head) * 2) * 1024 + lane * 16;
#pragma unroll
    for (int s = 0; s < 2; s++) {
      u32x4 qv;
#pragma unroll
      for (int j = 0; j < 4; j++) qv[j] = pack2(accq[mt][8 * s + 2 * j], accq[mt][8 * s + 2 * j + 1]);
      *(u32x4*)(qp + s * 1024) = qv;
      u32x4 pk, vv;
#pragma unroll
      for (int j = 0; j < 4; j++) {
        pk[j] = phi2(pack2(acck[mt][8 * s + 2 * j], acck[mt][8 * s + 2 * j + 1]));
        vv[j] = pack2(accv[mt][8 * s + 2 * j], accv[mt][8 * s + 2 * j + 1]);
      }
      bf16x8 pkf = __builtin_bit_cast(bf16x8, pk), vf = __builtin_bit_cast(bf16x8, vv);
      kv = MFMA32(pkf, vf, kv);
      zz = MFMA32(pkf, ones, zz);
    }
  }
  float* xb = (float*)(smem + 1 * K1_ASTG) + wn * 1280;
  if (wm == 1) {
#pragma unroll
    for (int rq = 0; rq < 4; rq++) {
      f32x4 v = {kv[4 * rq], kv[4 * rq + 1], kv[4 * rq + 2], kv[4 * rq + 3]};
      *(f32x4*)(xb + rq * 256 + lane * 4) = v;
    }
    if (r == 0) {
#pragma unroll
      for (int i = 0; i < 16; i++) xb[1024 + hh * 16 + i] = zz[i];
    }
  }
  BARRIER();
  if (wm == 0) {
    float* kp = (float*)(ws + WS_KVP) + (size_t)(head * 16 + rb) * 1024;
#pragma unroll
    for (int rq = 0; rq < 4; rq++) {
      f32x4 o = *(const f32x4*)(xb + rq * 256 + lane * 4);
      f32x4 v = {kv[4 * rq] + o[0], kv[4 * rq + 1] + o[1], kv[4 * rq + 2] + o[2], kv[4 * rq + 3] + o[3]};
      *(f32x4*)(kp + rq * 256 + lane * 4) = v;
    }
    if (r == 0) {
      float* zp = (float*)(ws + WS_ZP) + (size_t)(head * 16 + rb) * 32 + hh * 16;
#pragma unroll
      for (int i = 0; i < 16; i++) zp[i] = zz[i] + xb[1024 + hh * 16 + i];
    }
  }
}

__global__ __launch_bounds__(256) void k2_o(char* __restrict__ ws) {
  __shared__ __attribute__((aligned(16))) char sm[2048 + 64];
  const int tid = threadIdx.x, lane = tid & 63, wid = tid >> 6;
  const int h = blockIdx.x & 31, rg = blockIdx.x >> 5;
  const int hh = lane >> 5;
  u32x4 qf[2][2];
#pragma unroll
  for (int t = 0; t < 2; t++)
#pragma unroll
    for (int s = 0; s < 2; s++) {
      int rt = rg * 8 + wid * 2 + t;
      qf[t][s] = *(const u32x4*)(ws + WS_Q + ((size_t)(rt * 32 + h) * 2 + s) * 1024 + lane * 16);
    }
  const float* kp = (const float*)(ws + WS_KVP) + (size_t)h * 16 * 1024 + wid * 256 + lane * 4;
  f32x4 acc = {0, 0, 0, 0};
#pragma unroll
  for (int rb = 0; rb < 16; rb++) acc += *(const f32x4*)(kp + rb * 1024);
  float zs = 0;
  if (tid < 32) {
    const float* zp = (const float*)(ws + WS_ZP) + (size_t)h * 16 * 32 + tid;
#pragma unroll
    for (int rb = 0; rb < 16; rb++) zs += zp[rb * 32];
  }
#pragma unroll
  for (int t = 0; t < 2; t++)
#pragma unroll
    for (int s = 0; s < 2; s++)
#pragma unroll
      for (int j = 0; j < 4; j++) qf[t][s][j] = phi2(qf[t][s][j]);
  u32x2 kvb = {pack2(acc[0], acc[1]), pack2(acc[2], acc[3])};
  *(u32x2*)(sm + (wid >> 1) * 1024 + lane * 16 + (wid & 1) * 8) = kvb;
  if (tid < 32) *(u16*)(sm + 2048 + tid * 2) = (u16)(pack2(zs, zs) & 0xffff);
  __syncthreads();
  bf16x8 kvf[2], zf[2];
#pragma unroll
  for (int s = 0; s < 2; s++) {
    kvf[s] = *(const bf16x8*)(sm + s * 1024 + lane * 16);
    zf[s] = *(const bf16x8*)(sm + 2048 + hh * 32 + s * 16);
  }
#pragma unroll
  for (int t = 0; t < 2; t++) {
    f32x16 num, den;
#pragma unroll
    for (int i = 0; i < 16; i++) { num[i] = 0; den[i] = 0; }
#pragma unroll
    for (int s = 0; s < 2; s++) {
      bf16x8 b = __builtin_bit_cast(bf16x8, qf[t][s]);
      num = MFMA32(kvf[s], b, num);
      den = MFMA32(zf[s], b, den);
    }
    float d = rbf(rbf(den[0]) + 1e-6f);
    float rd = 1.0f / d;
    int rt = rg * 8 + wid * 2 + t;
#pragma unroll
    for (int s = 0; s < 2; s++) {
      u32x4 ov;
#pragma unroll
      for (int j = 0; j < 4; j++) {
        unsigned n2 = pack2(num[8 * s + 2 * j], num[8 * s + 2 * j + 1]);
        ov[j] = pack2(lo2f(n2) * rd, hi2f(n2) * rd);
      }
      *(u32x4*)(ws + WS_O + ((size_t)(rt * 32 + h) * 2 + s) * 1024 + lane * 16) = ov;
    }
  }
}

constexpr int K3_LDS = 131072;
__global__ __launch_bounds__(512, 2) void k3_out(const u16* __restrict__ wo, const char* __restrict__ ws,
                                                 u16* __restrict__ out) {
  extern __shared__ __attribute__((aligned(16))) char smem[];
  const int tid = threadIdx.x, lane = tid & 63;
  const int wid = __builtin_amdgcn_readfirstlane(tid >> 6);
  const int bid = blockIdx.x;
  const int xcd = bid & 7, idx = bid >> 3;
  const int rb = (xcd & 3) * 4 + (idx & 3);
  const int cb = (xcd >> 2) * 8 + (idx >> 2);
  const int m = wid & 3, kh = wid >> 2;
  const int rt = rb * 4 + m;
  const int r8 = lane >> 3, cp = lane & 7;
  const int hh = lane >> 5;
  const char* op = ws + WS_O + ((size_t)(rt * 32 + kh * 16) * 2) * 1024 + lane * 16;
  const unsigned woff = (unsigned)(r8 * 2048 + cb * 128 + ((cp ^ (((r8 >> 1) & 1) << 2)) * 16));
  u32x4 of[4][8];
#pragma unroll
  for (int p = 0; p < 4; p++) {
#pragma unroll
    for (int i = 0; i < 8; i++)
      asm volatile("global_load_dwordx4 %0, %1, off offset:0" : "=v"(of[p][i]) : "v"(op + (p * 8 + i) * 1024) : "memory");
#pragma unroll
    for (int i = 0; i < 4; i++) {
      int krow = (wid >> 2) * 512 + p * 128 + (wid & 3) * 32 + i * 8;
      __builtin_amdgcn_global_load_lds((const unsigned*)((const char*)wo + (size_t)krow * 2048 + woff),
                                       (LAS unsigned*)(smem + krow * 128), 16, 0, 0);
    }
  }
  const int blk = (lane >> 4) & 1, q4 = (lane & 15) >> 2, p4 = lane & 3;
  const int sw = (q4 >> 1) & 1;
  const char* bb0 = smem + (kh * 512 + 4 * hh + q4) * 128 + ((0 ^ sw) * 64) + blk * 32 + p4 * 8;
  const char* bb1 = smem + (kh * 512 + 4 * hh + q4) * 128 + ((1 ^ sw) * 64) + blk * 32 + p4 * 8;
  f32x16 acc[2];
#pragma unroll
  for (int i = 0; i < 16; i++) { acc[0][i] = 0; acc[1][i] = 0; }
#pragma unroll
  for (int p = 0; p < 4; p++) {
    if (p == 0) asm volatile("s_waitcnt vmcnt(36)" : "+v"(of[0][0]), "+v"(of[0][1]), "+v"(of[0][2]), "+v"(of[0][3]), "+v"(of[0][4]), "+v"(of[0][5]), "+v"(of[0][6]), "+v"(of[0][7]) :: "memory");
    if (p == 1) asm volatile("s_waitcnt vmcnt(24)" : "+v"(of[1][0]), "+v"(of[1][1]), "+v"(of[1][2]), "+v"(of[1][3]), "+v"(of[1][4]), "+v"(of[1][5]), "+v"(of[1][6]), "+v"(of[1][7]) :: "memory");
    if (p == 2) asm volatile("s_waitcnt vmcnt(12)" : "+v"(of[2][0]), "+v"(of[2][1]), "+v"(of[2][2]), "+v"(of[2][3]), "+v"(of[2][4]), "+v"(of[2][5]), "+v"(of[2][6]), "+v"(of[2][7]) :: "memory");
    if (p == 3) asm volatile("s_waitcnt vmcnt(0)" : "+v"(of[3][0]), "+v"(of[3][1]), "+v"(of[3][2]), "+v"(of[3][3]), "+v"(of[3][4]), "+v"(of[3][5]), "+v"(of[3][6]), "+v"(of[3][7]) :: "memory");
    BARRIER();
#pragma unroll
    for (int i = 0; i < 8; i++) {
      int ro = (p * 128 + i * 16) * 128;
      bf16x8 w0 = trfrag(bb0, ro, ro + 1024);
      bf16x8 w1 = trfrag(bb1, ro, ro + 1024);
      bf16x8 b = __builtin_bit_cast(bf16x8, of[p][i]);
      acc[0] = MFMA32(w0, b, acc[0]);
      acc[1] = MFMA32(w1, b, acc[1]);
    }
  }
  BARRIER();
  float* xb = (float*)smem + (m * 2 + kh) * 1024;
  {
    const f32x16& g = acc[kh ^ 1];
#pragma unroll
    for (int rq = 0; rq < 4; rq++) {
      f32x4 v;
#pragma unroll
      for (int j = 0; j < 4; j++) v[j] = kh ? acc[0][4 * rq + j] : acc[1][4 * rq + j];
      *(f32x4*)(xb + rq * 256 + lane * 4) = v;
    }
    (void)g;
  }
  BARRIER();
  const float* xr = (const float*)smem + (m * 2 + (kh ^ 1)) * 1024;
  const int srow = rt * 32 + (lane & 31);
  u16* orow = out + (size_t)srow * 2048 + cb * 64 + kh * 32 + 4 * hh;
#pragma unroll
  for (int rq = 0; rq < 4; rq++) {
    f32x4 o = *(const f32x4*)(xr + rq * 256 + lane * 4);
    float v0 = (kh ? acc[1][4 * rq + 0] : acc[0][4 * rq + 0]) + o[0];
    float v1 = (kh ? acc[1][4 * rq + 1] : acc[0][4 * rq + 1]) + o[1];
    float v2 = (kh ? acc[1][4 * rq + 2] : acc[0][4 * rq + 2]) + o[2];
    float v3 = (kh ? acc[1][4 * rq + 3] : acc[0][4 * rq + 3]) + o[3];
    u32x2 pv = {pack2(v0, v1), pack2(v2, v3)};
    *(u32x2*)(orow + 8 * rq) = pv;
  }
}

constexpr int S_U = 0;
constexpr int S_W0 = 4608;
constexpr int S_W1 = S_W0 + 6912;
constexpr int S_W2 = S_W1 + 2080;
constexpr int S_W3 = S_W2 + 1040;
constexpr int S_CH = S_W3 + 528;
constexpr int S_KP = 4 * S_CH;
constexpr int S_PAR = S_KP + 65536;
constexpr int S_K2048 = S_PAR + 8192;
constexpr int S_LDS = S_K2048 + 64;

DI int pos_idx(int l) {
  if (l < 256) return l;
  int s = (l < 512) ? 1 : (l < 1024) ? 2 : 3;
  int half = 1 << (s - 1);
  int i = (l + half - 1 + ((l >> s) & 1)) >> s;
  if (i == 256) return (s == 3) ? 1024 : (s + 1) * 256 + 128;
  return s * 256 + i;
}
DI int rhe(int l, int s) { int half = 1 << (s - 1); return (l + half - 1 + ((l >> s) & 1)) >> s; }

__global__ __launch_bounds__(512, 2) void k_ssm(const u16* __restrict__ u, const u16* __restrict__ lar,
                                                const u16* __restrict__ aim, const u16* __restrict__ cre,
                                                const u16* __restrict__ cim, const u16* __restrict__ ldt,
                                                const u16* __restrict__ dsk, u16* __restrict__ out) {
  extern __shared__ __attribute__((aligned(16))) char smem[];
  const int tid = threadIdx.x, lane = tid & 63;
  const int wid = __builtin_amdgcn_readfirstlane(tid >> 6);
  const int bid = blockIdx.x;
  const int d0 = ((bid & 7) * 32 + (bid >> 3)) * 4;
  const int ch = wid >> 1, hf = wid & 1;
  const int d = d0 + ch;
  char* cbase = smem + ch * S_CH;
  u32x2 ur[4];
#pragma unroll
  for (int i = 0; i < 4; i++) ur[i] = *(const u32x2*)(u + (size_t)(tid * 4 + i) * 1024 + d0);
  {
    const int n = hf * 64 + lane;
    float dt = rbf(__expf(b2f(ldt[d])));
    float ar = -rbf(__expf(b2f(lar[d * 128 + n]))) * dt;
    float ai = b2f(aim[d * 128 + n]) * dt;
    float cr = b2f(cre[d * 128 + n]) * dt * 2.f, ci = b2f(cim[d * 128 + n]) * dt * 2.f;
    float arl = ar * 1.44269504f, air = ai * 0.15915494f;
    f32x4 pv = {arl, air, cr, ci};
    *(f32x4*)(smem + S_PAR + (ch * 128 + n) * 16) = pv;
    float e = __builtin_amdgcn_exp2f(arl * 2048.f);
    float ph = __builtin_amdgcn_fractf(air * 2048.f);
    float t = e * (cr * __builtin_amdgcn_cosf(ph) - ci * __builtin_amdgcn_sinf(ph));
#pragma unroll
    for (int o = 32; o > 0; o >>= 1) t += __shfl_xor(t, o);
    if (lane == 0) *(float*)(smem + S_K2048 + wid * 4) = t;
  }
  const int idx = lane & 15, g = lane >> 4;
  const float fidx = (float)idx;
  f32x4 D[4];
#pragma unroll
  for (int s = 0; s < 4; s++) D[s] = f32x4{0, 0, 0, 0};
#pragma unroll 1
  for (int ks = 0; ks < 4; ks++) {
    u32x4 Ef[4], Gf[4];
#pragma unroll
    for (int e = 0; e < 4; e++) {
      f32x4 pv = *(const f32x4*)(smem + S_PAR + (ch * 128 + hf * 64 + ks * 16 + g * 4 + e) * 16);
      float mg = __builtin_amdgcn_exp2f(pv[0] * fidx);
      float ph = __builtin_amdgcn_fractf(pv[1] * fidx);
      float er = mg * __builtin_amdgcn_cosf(ph), ei = mg * __builtin_amdgcn_sinf(ph);
#pragma unroll
      for (int s = 0; s < 4; s++) {
        Ef[s][e] = pack2(er, ei);
        float nr = er * er - ei * ei, ni = 2.f * er * ei;
        er = nr; ei = ni;
      }
      float gr = pv[2] * er - pv[3] * ei, gi = pv[2] * ei + pv[3] * er;
#pragma unroll
      for (int s = 0; s < 4; s++) {
        Gf[s][e] = pack2(gr, -gi);
        if (s < 3) {
          float ngr = gr * er - gi * ei, ngi = gr * ei + gi * er;
          gr = ngr; gi = ngi;
          if (s < 2) { float nr = er * er - ei * ei, ni = 2.f * er * ei; er = nr; ei = ni; }
        }
      }
    }
#pragma unroll
    for (int s = 0; s < 4; s++)
      D[s] = MFMA16(__builtin_bit_cast(bf16x8, Gf[s]), __builtin_bit_cast(bf16x8, Ef[s]), D[s]);
  }
  {
    float* kp = (float*)(smem + S_KP) + (ch * 2 + hf) * 1024;
#pragma unroll
    for (int s = 0; s < 4; s++)
#pragma unroll
      for (int j = 0; j < 4; j++) kp[s * 256 + (4 * g + j) * 16 + idx] = D[s][j];
  }
#pragma unroll
  for (int c = 0; c < 4; c++) {
    unsigned sel = (c & 1) ? 0x07060302u : 0x05040100u;
    u32x2 v = {__builtin_amdgcn_perm(ur[1][c >> 1], ur[0][c >> 1], sel), __builtin_amdgcn_perm(ur[3][c >> 1], ur[2][c >> 1], sel)};
    *(u32x2*)(smem + c * S_CH + S_U + 512 + tid * 8) = v;
  }
  if (tid < 128) *(u32x4*)(smem + (tid >> 5) * S_CH + S_U + (tid & 31) * 16) = u32x4{0, 0, 0, 0};
  __syncthreads();
  {
    const int t = hf * 64 + lane;
    const float* k0 = (const float*)(smem + S_KP) + (ch * 2) * 1024;
    const float k2048 = *(const float*)(smem + S_K2048 + ch * 8) + *(const float*)(smem + S_K2048 + ch * 8 + 4);
    auto kd = [&](int ci) -> float { return ci >= 1024 ? k2048 : k0[ci] + k0[1024 + ci]; };
    auto kx = [&](int l) -> float { return l < 0 ? 0.f : kd(pos_idx(l)); };
    for (int it = t; it < 394; it += 128) {
      int l, xlo, xhi, ebase;
      if (it < 302) { l = it - 14; xlo = -8; xhi = 287; ebase = 16; }
      else if (it < 348) { l = it - 302 + 498; xlo = 504; xhi = 543; ebase = 336 - 512; }
      else { l = it - 348 + 1010; xlo = 1016; xhi = 1055; ebase = 400 - 1024; }
      unsigned P = pack2(kx(l), kx(l - 1));
#pragma unroll
      for (int q = 0; q < 4; q++) {
        int xx = l + 2 * q;
        if (xx >= xlo && xx <= xhi) {
          int e = xx + ebase;
          e ^= ((e >> 4) & 1) << 3;
          *(unsigned*)(cbase + S_W0 + e * 16 + q * 4) = P;
        }
      }
    }
    for (int it = t; it < 129; it += 128) {
      int i = 128 + it;
#pragma unroll
      for (int s = 1; s <= 3; s++) {
        auto V = [&](int ii) -> float { return ii < 128 ? 0.f : (ii == 256 ? (s == 3 ? k2048 : kd((s + 1) * 256 + 128)) : kd(s * 256 + ii)); };
        if (s == 1) { u32x4 v = {pack2(V(i), V(i - 1)), pack2(V(i - 2), V(i - 3)), pack2(V(i - 4), V(i - 5)), 0u}; *(u32x4*)(cbase + S_W1 + it * 16) = v; }
        if (s == 2) { u32x2 v = {pack2(V(i), V(i - 1)), pack2(V(i - 2), V(i - 3))}; *(u32x2*)(cbase + S_W2 + it * 8) = v; }
        if (s == 3) { *(unsigned*)(cbase + S_W3 + it * 4) = pack2(V(i), V(i - 1)); }
      }
    }
  }
  __syncthreads();
  {
    const int r = idx;
    const int y = 16 * (g >> 1) - 8 * (g & 1) + r;
    int e0 = y + 16; e0 ^= ((e0 >> 4) & 1) << 3;
    const char* a0 = cbase + S_W0 + e0 * 16;
    unsigned sel[3][4];
    int ioff[3];
#pragma unroll
    for (int s = 1; s <= 3; s++) {
      int it = rhe(y + 64, s);
      ioff[s - 1] = it - (64 >> s);
#pragma unroll
      for (int q = 0; q < 4; q++) {
        int ea = it - rhe(y + 64 - 2 * q, s), eb = it - rhe(y + 64 - 2 * q - 1, s);
        int wbase = (s == 1 && q >= 2) ? 2 : 0;
        ea -= wbase; eb -= wbase;
        sel[s - 1][q] = (unsigned)((2 * ea) | ((2 * ea + 1) << 8) | ((2 * eb) << 16) | ((2 * eb + 1) << 24));
      }
    }
    const char* a1 = cbase + S_W1 + (ioff[0] - 128) * 16;
    const char* a2 = cbase + S_W2 + (ioff[1] - 128) * 8;
    const char* a3 = cbase + S_W3 + (ioff[2] - 128) * 4;
    const int coff = (g == 0) ? 0 : (g == 1) ? 1 : (g == 2) ? -2 : -1;
    const char* ub = cbase + S_U + 512 + (2 * idx + coff) * 16;
    f32x4 Y[8];
#pragma unroll
    for (int c = 0; c < 8; c++) Y[c] = f32x4{0, 0, 0, 0};
#pragma unroll
    for (int s4 = 0; s4 < 4; s4++) {
      const int s = hf * 4 + s4;
      bf16x8 Bf[8];
#pragma unroll
      for (int e = 0; e < 8; e++) Bf[e] = *(const bf16x8*)(ub + (8 * e - s) * 64);
#pragma unroll
      for (int mh = 0; mh < 8; mh++) {
        const int m = 8 * mh + s;
        bf16x8 A;
        if (mh == 0) A = *(const bf16x8*)(a0 + m * 512);
        else if (s4 == 0 && (mh == 1 || mh == 2 || mh == 4) && hf == 0) {
          int eo = (mh == 1) ? 256 : (mh == 2) ? 320 : 384;
          A = *(const bf16x8*)(a0 + eo * 16);
        } else if (mh == 1) {
          u32x4 wv = *(const u32x4*)(a1 + m * 16 * 16);
          A = mk8(__builtin_amdgcn_perm(wv[1], wv[0], sel[0][0]), __builtin_amdgcn_perm(wv[1], wv[0], sel[0][1]),
                  __builtin_amdgcn_perm(wv[2], wv[1], sel[0][2]), __builtin_amdgcn_perm(wv[2], wv[1], sel[0][3]));
        } else if (mh < 4) {
          u32x2 wv = *(const u32x2*)(a2 + m * 8 * 8);
          A = mk8(__builtin_amdgcn_perm(wv[1], wv[0], sel[1][0]), __builtin_amdgcn_perm(wv[1], wv[0], sel[1][1]),
                  __builtin_amdgcn_perm(wv[1], wv[0], sel[1][2]), __builtin_amdgcn_perm(wv[1], wv[0], sel[1][3]));
        } else {
          unsigned wv = *(const unsigned*)(a3 + m * 4 * 4);
          A = mk8(__builtin_amdgcn_perm(wv, wv, sel[2][0]), __builtin_amdgcn_perm(wv, wv, sel[2][1]),
                  __builtin_amdgcn_perm(wv, wv, sel[2][2]), __builtin_amdgcn_perm(wv, wv, sel[2][3]));
        }
#pragma unroll
        for (int c = mh; c < 8; c++) Y[c] = MFMA16(A, Bf[c - mh], Y[c]);
      }
    }
    float* yp = (float*)(smem + S_KP) + (ch * 2 + hf) * 2048;
#pragma unroll
    for (int c = 0; c < 8; c++) *(f32x4*)(yp + c * 256 + idx * 16 + g * 4) = Y[c];
  }
  __syncthreads();
  {
    float dk[4];
#pragma unroll
    for (int c = 0; c < 4; c++) dk[c] = b2f(dsk[d0 + c]);
    f32x4 ys[4];
#pragma unroll
    for (int c = 0; c < 4; c++) {
      const float* yp = (const float*)(smem + S_KP) + (c * 2) * 2048 + tid * 4;
      ys[c] = *(const f32x4*)(yp) + *(const f32x4*)(yp + 2048);
    }
#pragma unroll
    for (int i = 0; i < 4; i++) {
      float o[4];
#pragma unroll
      for (int c = 0; c < 4; c++) {
        float uv = (c & 1) ? hi2f(ur[i][c >> 1]) : lo2f(ur[i][c >> 1]);
        o[c] = rbf(ys[c][i]) + rbf(uv * dk[c]);
      }
      u32x2 pv = {pack2(o[0], o[1]), pack2(o[2], o[3])};
      *(u32x2*)(out + (size_t)(tid * 4 + i) * 2048 + 1024 + d0) = pv;
    }
  }
}

extern "C" void kernel_launch(void* const* d_in, const int* in_sizes, int n_in, void* d_out, int out_size,
                              void* d_ws, size_t ws_size, hipStream_t stream) {
  const u16* x = (const u16*)d_in[0];
  const u16* wqkv = (const u16*)d_in[1];
  const u16* wo = (const u16*)d_in[2];
  const u16* u = (const u16*)d_in[3];
  u16* out = (u16*)d_out;
  char* ws = (char*)d_ws;
  (void)hipFuncSetAttribute((const void*)k1_qkv, hipFuncAttributeMaxDynamicSharedMemorySize, K1_LDS);
  (void)hipFuncSetAttribute((const void*)k3_out, hipFuncAttributeMaxDynamicSharedMemorySize, K3_LDS);
  (void)hipFuncSetAttribute((const void*)k_ssm, hipFuncAttributeMaxDynamicSharedMemorySize, S_LDS);
  k1_qkv<<<256, 512, K1_LDS, stream>>>(x, wqkv, ws);
  k2_o<<<256, 256, 0, stream>>>(ws);
  k3_out<<<256, 512, K3_LDS, stream>>>(wo, ws, out);
  k_ssm<<<256, 512, S_LDS, stream>>>(u, (const u16*)d_in[4], (const u16*)d_in[5], (const u16*)d_in[6],
                                     (const u16*)d_in[7], (const u16*)d_in[8], (const u16*)d_in[9], out);
}
```

```cpp
#include <hip/hip_runtime.h>
#include <hip/hip_bf16.h>
#include <stdint.h>

typedef unsigned short u16;
typedef unsigned long long u64;
typedef __attribute__((ext_vector_type(8))) __bf16 bf16x8;
typedef __attribute__((ext_vector_type(2))) __bf16 bf16x2;
typedef __attribute__((ext_vector_type(4))) short s16x4;
typedef __attribute__((ext_vector_type(4))) float f32x4;
typedef __attribute__((ext_vector_type(2))) float f32x2;
typedef __attribute__((ext_vector_type(4))) unsigned u32x4;
typedef __attribute__((ext_vector_type(2))) unsigned u32x2;
#define LAS __attribute__((address_space(3)))
#if defined(__HIP_DEVICE_COMPILE__)
#define NOPK __attribute__((target("no-packed-fp32-ops")))
#else
#define NOPK
#endif
#define DI __device__ __forceinline__ NOPK

#define MFMA16(a, b, c) __builtin_amdgcn_mfma_f32_16x16x32_bf16((a), (b), (c), 0, 0, 0)
#define BARRIER() asm volatile("s_waitcnt lgkmcnt(0)\n\ts_barrier" ::: "memory")
#define WAIT_VM(n) asm volatile("s_waitcnt vmcnt(%0)" ::"n"(n) : "memory")
#define SB() __builtin_amdgcn_sched_barrier(0)
#define BC8(x) __builtin_bit_cast(bf16x8, (x))

DI unsigned pack2(float a, float b) {
  f32x2 v = {a, b};
  return __builtin_bit_cast(unsigned, __builtin_convertvector(v, bf16x2));
}
DI float lo2f(unsigned p) { return __builtin_bit_cast(float, p << 16); }
DI float hi2f(unsigned p) { return __builtin_bit_cast(float, p & 0xffff0000u); }
DI float rbf(float x) { return lo2f(pack2(x, x)); }
DI unsigned phi2(float x, float y) {
  unsigned p = pack2(x, y);
  float a = lo2f(p), b = hi2f(p);
  float ea = __builtin_amdgcn_exp2f(fminf(a, 0.f) * 1.44269504f) + fmaxf(a, 0.f);
  float eb = __builtin_amdgcn_exp2f(fminf(b, 0.f) * 1.44269504f) + fmaxf(b, 0.f);
  unsigned r = pack2(ea, eb);
  return pack2(lo2f(r) + 1e-6f, hi2f(r) + 1e-6f);
}
DI void glds16nt(const char* base, unsigned voff, unsigned ldsaddr) {
  asm volatile("s_mov_b32 m0, %0\n\ts_nop 0\n\tglobal_load_lds_dwordx4 %1, %2 nt" ::"s"(ldsaddr), "v"(voff), "s"(base) : "memory");
}
DI void glds16(const char* base, unsigned voff, unsigned ldsaddr) {
  asm volatile("s_mov_b32 m0, %0\n\ts_nop 0\n\tglobal_load_lds_dwordx4 %1, %2" ::"s"(ldsaddr), "v"(voff), "s"(base) : "memory");
}
template <int OFF> DI void st16_sc1(char* base, unsigned voff, u32x4 v) {
  asm volatile("global_store_dwordx4 %0, %1, %2 offset:%3 sc1" ::"v"(voff), "v"(v), "s"(base), "n"(OFF) : "memory");
}
template <int OFF> DI void ld16_sc1(u32x4& d, const char* base, unsigned voff) {
  asm volatile("global_load_dwordx4 %0, %1, %2 offset:%3 sc1" : "=v"(d) : "v"(voff), "s"(base), "n"(OFF) : "memory");
}
DI float dppadd(float v, const int ctrl_sel) {
  int iv = __builtin_bit_cast(int, v), r;
  if (ctrl_sel == 0) r = __builtin_amdgcn_update_dpp(0, iv, 0xB1, 0xf, 0xf, true);
  else if (ctrl_sel == 1) r = __builtin_amdgcn_update_dpp(0, iv, 0x4E, 0xf, 0xf, true);
  else if (ctrl_sel == 2) r = __builtin_amdgcn_update_dpp(0, iv, 0x141, 0xf, 0xf, true);
  else r = __builtin_amdgcn_update_dpp(0, iv, 0x140, 0xf, 0xf, true);
  return v + __builtin_bit_cast(float, r);
}

constexpr size_t WS_EPOCH = 0;
constexpr size_t WS_FLAG = 4096;
constexpr size_t WS_Z = 65536;
constexpr size_t WS_KVP = 1u << 20;
constexpr size_t WS_O = 4u << 20;
constexpr unsigned SENT = 0xFFFFFFFFu;
constexpr int SPIN_CAP = 1 << 17;

constexpr int ASTG = 16384, BSTG = 24576, BOFF = 3 * ASTG, RING = 3 * (ASTG + BSTG);
constexpr int XCH = 114688;
constexpr int SS = RING, SCH = 8704;
constexpr int MISC = SS + 4 * SCH;
constexpr int LDSZ = 163840;

#define STAMP(i)

DI void lds_wait(volatile LAS unsigned* f) {
  int spins = 0;
  while (*f == 0u) {
    __builtin_amdgcn_s_sleep(1);
    if (++spins > SPIN_CAP) break;
  }
}

__global__ __launch_bounds__(512, 2) NOPK void fused(const u16* __restrict__ x, const u16* __restrict__ w,
                                                     const u16* __restrict__ wo, const u16* __restrict__ u,
                                                     const u16* __restrict__ lar, const u16* __restrict__ aim,
                                                     const u16* __restrict__ cre, const u16* __restrict__ cim,
                                                     const u16* __restrict__ ldt, const u16* __restrict__ dsk,
                                                     u16* __restrict__ out, char* __restrict__ ws) {
  extern __shared__ __attribute__((aligned(16))) char smem[];
  LAS char* lds = (LAS char*)smem;
  const int tid = threadIdx.x, lane = tid & 63;
  const int wid = __builtin_amdgcn_readfirstlane(tid >> 6);
  const int bid = blockIdx.x;
  const int xcd = bid & 7, idx = bid >> 3;
  const int rb = (xcd & 1) * 8 + (idx & 7);
  const int cb = (xcd >> 1) * 4 + (idx >> 3);
  const int rb2 = (xcd & 3) * 4 + (idx & 3);
  const int cb2 = (xcd >> 2) * 8 + (idx >> 2);
  const int i16 = lane & 15, g = lane >> 4;
  volatile LAS unsigned* lflag = (volatile LAS unsigned*)(lds + MISC);
  STAMP(0);

  if (wid >= 4) {
    const int lw = wid & 3;
    const int r8 = lane >> 3, cp = lane & 7;
    const int d0 = (xcd * 32 + idx) * 4, dch = d0 + lw;
    unsigned p_lar, p_aim, p_cre, p_cim, p_ldt, p_dsk;
    {
      const unsigned po = (unsigned)(dch * 256 + lane * 4), so = (unsigned)(dch * 2);
      asm volatile("global_load_dword %0, %1, %2" : "=v"(p_lar) : "v"(po), "s"(lar) : "memory");
      asm volatile("global_load_dword %0, %1, %2" : "=v"(p_aim) : "v"(po), "s"(aim) : "memory");
      asm volatile("global_load_dword %0, %1, %2" : "=v"(p_cre) : "v"(po), "s"(cre) : "memory");
      asm volatile("global_load_dword %0, %1, %2" : "=v"(p_cim) : "v"(po), "s"(cim) : "memory");
      asm volatile("global_load_ushort %0, %1, %2" : "=v"(p_ldt) : "v"(so), "s"(ldt) : "memory");
      asm volatile("global_load_ushort %0, %1, %2" : "=v"(p_dsk) : "v"(so), "s"(dsk) : "memory");
    }
    unsigned aoff[4], boff[6];
#pragma unroll
    for (int ii = 0; ii < 4; ii++) {
      int row = (4 * lw + ii) * 8 + r8;
      int c = cp ^ ((row >> 1) & 7);
      aoff[ii] = (unsigned)((rb * 128 + row) * 2048 + c * 16);
    }
#pragma unroll
    for (int jj = 0; jj < 6; jj++) {
      int id = 6 * lw + jj;
      int sg = id >> 3, j = id & 7;
      int k = j * 8 + r8;
      int c = cp ^ ((((k >> 1) & 1) | (((k >> 3) & 1) << 1)) << 1);
      boff[jj] = (unsigned)(k * 6144 + (sg * 1024 + cb * 64) * 2 + c * 16);
    }
    const int r_ = idx & 7, c_ = idx >> 3;
    const int koff = 4 * ((r_ & 1) + 2 * (((r_ >> 1) ^ c_) & 1)) + (xcd & 3);
    auto issue = [&](int kt0) NOPK {
      const int kt = (kt0 + koff) & 15, st = kt0 % 3;
      const char* ap = (const char*)x + kt * 128;
      const char* bp = (const char*)w + (size_t)kt * (64 * 6144);
      const unsigned la = st * ASTG + lw * 4096, lb = BOFF + st * BSTG + lw * 6144;
#pragma unroll
      for (int ii = 0; ii < 4; ii++) glds16nt(ap, aoff[ii], la + ii * 1024);
#pragma unroll
      for (int jj = 0; jj < 6; jj++) glds16nt(bp, boff[jj], lb + jj * 1024);
    };
    const unsigned wooff = (unsigned)((8 * lw + r8) * 2048 + cb2 * 128 + ((cp ^ (((r8 >> 1) & 3) << 1)) * 16));
    auto issue_wo = [&](int s0, int s1) NOPK {
#pragma unroll
      for (int s = s0; s < s1; s++) {
        const int h = s < 14 ? s : s + 2;
        glds16((const char*)wo + h * 65536, wooff, s * 4096 + lw * 1024);
      }
    };
    STAMP(5);
    issue(0); issue(1); issue(2);
    SB();
    STAMP(6);
#define HPRIO 1
    __builtin_amdgcn_s_setprio(HPRIO);
#define TICK(kt, n)                                                     \
  SB();                                                                 \
  WAIT_VM(n);                                                           \
  BARRIER();                                                            \
  if ((kt) & 1) { STAMP(8 + ((kt) >> 1)); }                             \
  if ((kt) + 3 < 16) issue((kt) + 3);                                   \
  if ((kt) == 13) { issue_wo(4, 8); issue_wo(18, 24); }                 \
  if ((kt) == 14) { issue_wo(8, 12); issue_wo(24, 28); }                \
  if ((kt) == 15) { issue_wo(0, 4); issue_wo(12, 18); }                 \
  SB();

    const int cbase = SS + lw * SCH;
    const int ubase = cbase + 4608;
    *(LAS u32x2*)(lds + cbase + 4096 + lane * 8) = u32x2{0u, 0u};
    unsigned wadr[16];
#pragma unroll
    for (int r = 0; r < 16; r++) wadr[r] = (unsigned)(ubase + r * 256 + ((lane * 4) ^ (r * 16)));
    unsigned radr[4];
#pragma unroll
    for (int ks = 0; ks < 4; ks++) radr[ks] = (unsigned)(ubase + i16 * 256 + (((ks * 4 + g) ^ i16) * 16));
    asm volatile("s_waitcnt vmcnt(30)" : "+v"(p_lar), "+v"(p_aim), "+v"(p_cre), "+v"(p_cim), "+v"(p_ldt), "+v"(p_dsk)::"memory");
    STAMP(1);
    const float dt = rbf(__builtin_amdgcn_exp2f(lo2f(p_ldt) * 1.44269504f));
    f32x4 D[4];
#pragma unroll
    for (int s = 0; s < 4; s++) D[s] = f32x4{0, 0, 0, 0};
    u32x2 ur[8];
#define WR(row, re, im) *(LAS unsigned*)(lds + wadr[row]) = pack2(re, im)
#define CMUL(or_, oi_, ar_, ai_, br_, bi_) { float t_r = (ar_) * (br_) - (ai_) * (bi_), t_i = (ar_) * (bi_) + (ai_) * (br_); or_ = t_r; oi_ = t_i; }
#define CEXP(or_, oi_, sc)                                                         \
  {                                                                                \
    float mg = __builtin_amdgcn_exp2f(arl * (sc));                                 \
    float ph = __builtin_amdgcn_fractf(air * (sc));                                \
    or_ = mg * __builtin_amdgcn_cosf(ph); oi_ = mg * __builtin_amdgcn_sinf(ph);    \
  }
#define GMM(seg)                                                                   \
  _Pragma("unroll") for (int ks = 0; ks < 4; ks++) {                               \
    bf16x8 gf = *(const LAS bf16x8*)(lds + radr[ks]);                              \
    D[seg] = MFMA16(gf, Ef[ks], D[seg]);                                           \
  }
#define KGEN(hf, T0, T1, T2, T3, T4, T5)                                           \
  {                                                                                \
    const float a_l = (hf) ? hi2f(p_lar) : lo2f(p_lar), a_i = (hf) ? hi2f(p_aim) : lo2f(p_aim); \
    const float c_r = ((hf) ? hi2f(p_cre) : lo2f(p_cre)) * dt * 2.f, c_i = ((hf) ? hi2f(p_cim) : lo2f(p_cim)) * dt * 2.f; \
    const float arl = -rbf(__builtin_amdgcn_exp2f(a_l * 1.44269504f)) * dt * 1.44269504f;       \
    const float air = a_i * dt * 0.15915494f;                                      \
    float z1r, z1i, z16r, z16i, zHr, zHi;                                          \
    CEXP(z1r, z1i, 1.f) CEXP(z16r, z16i, 16.f) CEXP(zHr, zHi, 256.f)               \
    T0                                                                             \
    bf16x8 Ef[4];                                                                  \
    {                                                                              \
      float er = z16r, ei = -z16i;                                                 \
      *(LAS unsigned*)(lds + wadr[0]) = 0x00003f80u;                               \
      _Pragma("unroll") for (int j = 1; j < 16; j++) {                             \
        WR(j, er, ei);                                                             \
        if (j < 15) CMUL(er, ei, er, ei, z16r, -z16i)                              \
      }                                                                            \
      _Pragma("unroll") for (int ks = 0; ks < 4; ks++) Ef[ks] = *(const LAS bf16x8*)(lds + radr[ks]); \
    }                                                                              \
    T1                                                                             \
    {                                                                              \
      float gr = c_r, gi = c_i;                                                    \
      _Pragma("unroll") for (int j = 0; j < 16; j++) {                             \
        WR(j, gr, gi);                                                             \
        if (j < 15) CMUL(gr, gi, gr, gi, z1r, z1i)                                 \
      }                                                                            \
      GMM(0)                                                                       \
    }                                                                              \
    T2                                                                             \
    float hr, hi_, zr, zi;                                                         \
    CMUL(hr, hi_, c_r, c_i, zHr, zHi)                                              \
    CMUL(zr, zi, z1r, z1i, z1r, z1i)                                               \
    {                                                                              \
      float gr = hr, gi = hi_;                                                     \
      _Pragma("unroll") for (int j = 0; j < 9; j++) {                              \
        unsigned pv = pack2(gr, gi);                                               \
        if (j < 8) *(LAS unsigned*)(lds + wadr[((j >> 1) * 4 + (j & 1)) & 15]) = pv; \
        if (j > 0 && !(j & 1)) *(LAS unsigned*)(lds + wadr[((j >> 1) * 4 - 2) & 15]) = pv; \
        if (j < 8) CMUL(gr, gi, gr, gi, zr, zi)                                    \
      }                                                                            \
      GMM(1)                                                                       \
    }                                                                              \
    T3                                                                             \
    CMUL(zr, zi, zr, zi, zr, zi)                                                   \
    _Pragma("unroll") for (int b = 0; b < 2; b++) {                                \
      CMUL(hr, hi_, hr, hi_, zHr, zHi)                                             \
      float gr = hr, gi = hi_;                                                     \
      _Pragma("unroll") for (int j = 0; j < 5; j++) {                              \
        unsigned pv = pack2(gr, gi);                                               \
        if (j <= 2) *(LAS unsigned*)(lds + wadr[b * 8 + j]) = pv;                  \
        if (j >= 2) *(LAS unsigned*)(lds + wadr[b * 8 + 4 + j - 2]) = pv;          \
        if (j < 4) CMUL(gr, gi, gr, gi, zr, zi)                                    \
      }                                                                            \
    }                                                                              \
    GMM(2)                                                                         \
    T4                                                                             \
    CMUL(zr, zi, zr, zi, zr, zi)                                                   \
    _Pragma("unroll") for (int b = 0; b < 4; b++) {                                \
      CMUL(hr, hi_, hr, hi_, zHr, zHi)                                             \
      float gr = hr, gi = hi_;                                                     \
      _Pragma("unroll") for (int j = 0; j < 3; j++) {                              \
        WR(b * 4 + j, gr, gi);                                                     \
        if (j < 2) CMUL(gr, gi, gr, gi, zr, zi)                                    \
      }                                                                            \
    }                                                                              \
    GMM(3)                                                                         \
    T5                                                                             \
  }
#define FIRSTBAR SB(); STAMP(7); WAIT_VM(20); BARRIER(); SB();
#define ULOAD                                                                      \
  {                                                                                \
    const unsigned uo = (unsigned)((lw * 64 + lane) * 8 * 2048 + d0 * 2);          \
    _Pragma("unroll") for (int i = 0; i < 8; i++)                                  \
      asm volatile("global_load_dwordx2 %0, %1, %2" : "=v"(ur[i]) : "v"(uo), "s"((const char*)u + i * 2048) : "memory"); \
    SB();                                                                          \
  }
    KGEN(0, , FIRSTBAR, TICK(0, 10), TICK(1, 10), TICK(2, 10), TICK(3, 10))
    KGEN(1, TICK(4, 10), TICK(5, 10), TICK(6, 10), TICK(7, 10) ULOAD, TICK(8, 18), TICK(9, 18))
    {
      const int c16 = i16 * 16;
      *(LAS u32x2*)(lds + cbase + 2 * (2044 - c16 - 4 * g)) = u32x2{pack2(D[0][3], D[0][2]), pack2(D[0][1], D[0][0])};
      *(LAS u32x2*)(lds + cbase + 2 * (2044 - 256 - c16 - 4 * g)) = u32x2{pack2(D[1][2], D[1][1]), pack2(D[1][0], D[1][0])};
      *(LAS u32x4*)(lds + cbase + 2 * (2040 - 512 - 256 * (g >> 1) - c16 - 8 * (g & 1))) =
          u32x4{pack2(D[2][2], D[2][2]), pack2(D[2][1], D[2][1]), pack2(D[2][1], D[2][0]), pack2(D[2][0], D[2][0])};
      unsigned cc = pack2(D[3][2], D[3][2]), bb = pack2(D[3][1], D[3][1]), ba = pack2(D[3][1], D[3][0]), aa = pack2(D[3][0], D[3][0]);
      const int o3 = cbase + 2 * (2032 - 1024 - 256 * g - c16);
      *(LAS u32x4*)(lds + o3) = u32x4{cc, cc, bb, bb};
      *(LAS u32x4*)(lds + o3 + 16) = u32x4{bb, ba, aa, aa};
    }
    TICK(10, 10)
    asm volatile("" : "+v"(ur[0]), "+v"(ur[1]), "+v"(ur[2]), "+v"(ur[3]), "+v"(ur[4]), "+v"(ur[5]), "+v"(ur[6]), "+v"(ur[7])::"memory");
#pragma unroll
    for (int c = 0; c < 4; c++) {
      const unsigned sel = (c & 1) ? 0x07060302u : 0x05040100u;
      u32x4 v;
#pragma unroll
      for (int q = 0; q < 4; q++) v[q] = __builtin_amdgcn_perm(ur[2 * q + 1][c >> 1], ur[2 * q][c >> 1], sel);
      *(LAS u32x4*)(lds + SS + c * SCH + 4608 + (lw * 64 + lane) * 16) = v;
    }
    TICK(11, 10)
    TICK(12, 10)
    TICK(13, 10)
    TICK(14, 10)
    TICK(15, 63)
    __builtin_amdgcn_s_setprio(0);
    STAMP(2);
    {
      const int coff = (g == 0) ? 0 : (g == 1) ? 1 : (g == 2) ? -2 : -1;
      const int y = i16 - 8 * coff;
      const int x0 = 2047 - y;
      const unsigned shift = (unsigned)(x0 & 1) * 2u;
      unsigned kad = (unsigned)(cbase + (x0 >> 1) * 4 - 512 * 7);
      unsigned uad = (unsigned)(ubase + 32 * i16 + 16 * coff);
      f32x4 Y[8];
#pragma unroll
      for (int c = 0; c < 8; c++) Y[c] = f32x4{0, 0, 0, 0};
      unsigned ar[2][8][5];
      bf16x8 bf[2][8];
#define CLOAD(set, soff)                                                                       \
  _Pragma("unroll") for (int e = 0; e < 8; e++) bf[set][e] = *(const LAS bf16x8*)(lds + uad + 512 * e + (soff)); \
  _Pragma("unroll") for (int mh = 0; mh < 8; mh++)                                            \
    _Pragma("unroll") for (int q = 0; q < 5; q++) ar[set][mh][q] = *(const LAS unsigned*)(lds + kad + 512 * (7 - mh) + 4 * q + (soff));
#define CCOMP(set)                                                                             \
  _Pragma("unroll") for (int mh = 0; mh < 8; mh++) {                                          \
    u32x4 av;                                                                                  \
    _Pragma("unroll") for (int q = 0; q < 4; q++) av[q] = __builtin_amdgcn_alignbyte(ar[set][mh][q + 1], ar[set][mh][q], shift); \
    _Pragma("unroll") for (int c = mh; c < 8; c++) Y[c] = MFMA16(BC8(av), bf[set][c - mh], Y[c]); \
  }
      kad -= 128; uad -= 128;
      CLOAD(0, 128)
#pragma unroll 1
      for (int it = 0; it < 4; it++) {
        SB();
        CLOAD(1, 64)
        SB();
        CCOMP(0)
        SB();
        CLOAD(0, 0)
        SB();
        CCOMP(1)
        kad -= 128; uad -= 128;
        if (it == 0) {
          WAIT_VM(0);
          if (lane == 0) lflag[8 + lw] = 1u;
        }
      }
      SB();
      STAMP(3);
      asm volatile("s_waitcnt lgkmcnt(0)" ::: "memory");
      {
        const int h = (lw >> 1) * 16 + 14 + (lw & 1);
        const unsigned wo2 = wooff - (unsigned)(8 * lw * 2048);
#pragma unroll
        for (int j = 0; j < 4; j++) glds16((const char*)wo + h * 65536 + j * 16384, wo2, cbase + j * 1024);
      }
      const float dk = lo2f(p_dsk);
#pragma unroll
      for (int c = 0; c < 8; c++) {
        LAS u32x2* up = (LAS u32x2*)(lds + ubase + c * 512 + i16 * 32 + g * 8);
        u32x2 uu = *up;
        unsigned y01 = pack2(Y[c][0], Y[c][1]), y23 = pack2(Y[c][2], Y[c][3]);
        unsigned s01 = pack2(lo2f(uu[0]) * dk, hi2f(uu[0]) * dk), s23 = pack2(lo2f(uu[1]) * dk, hi2f(uu[1]) * dk);
        *up = u32x2{pack2(lo2f(y01) + lo2f(s01), hi2f(y01) + hi2f(s01)), pack2(lo2f(y23) + lo2f(s23), hi2f(y23) + hi2f(s23))};
      }
      asm volatile("" ::: "memory");
      if (lane == 0) lflag[16 + lw] = 1u;
      WAIT_VM(0);
      if (lane == 0) lflag[12 + lw] = 1u;
      lds_wait(lflag + 16); lds_wait(lflag + 17); lds_wait(lflag + 18); lds_wait(lflag + 19);
      asm volatile("" ::: "memory");
      u32x4 yv[4];
#pragma unroll
      for (int c = 0; c < 4; c++) yv[c] = *(const LAS u32x4*)(lds + SS + c * SCH + 4608 + (lw * 64 + lane) * 16);
      u16* op = out + (size_t)((lw * 64 + lane) * 8) * 2048 + 1024 + d0;
#pragma unroll
      for (int i = 0; i < 8; i++) {
        const unsigned sel = (i & 1) ? 0x07060302u : 0x05040100u;
        u32x2 v = {__builtin_amdgcn_perm(yv[1][i >> 1], yv[0][i >> 1], sel), __builtin_amdgcn_perm(yv[3][i >> 1], yv[2][i >> 1], sel)};
        *(u32x2*)(op + (size_t)i * 2048) = v;
      }
      STAMP(4);
    }
    return;
  }

#define CPRIO 0
  __builtin_amdgcn_s_setprio(CPRIO);
  const int wm = wid & 1, wn = wid >> 1;
  u32x2 epoch;
  asm volatile("global_load_dwordx2 %0, %1, %2 sc1\n\ts_waitcnt vmcnt(0)" : "=v"(epoch) : "v"(0u), "s"(ws + WS_EPOCH) : "memory");
  u32x2 tag;
  {
    u64 e = ((u64)(unsigned)__builtin_amdgcn_readfirstlane(epoch[1]) << 32 | (unsigned)__builtin_amdgcn_readfirstlane(epoch[0])) + 0x9E3779B97F4A7C15ull;
    e = (e ^ (e >> 30)) * 0xBF58476D1CE4E5B9ull;
    e = (e ^ (e >> 27)) * 0x94D049BB133111EBull;
    e ^= e >> 31;
    tag = u32x2{(unsigned)e, (unsigned)(e >> 32)};
  }
  if (tid < 32) lflag[tid] = 0u;
  unsigned flgoff;
  {
    const int frb = lane < 32 ? i16 : rb2;
    const int fcb = lane < 32 ? cb : 8 * wn + (i16 >> 1);
    const int fw = lane < 16 ? wid : lane < 32 ? 2 * wn : wm + 2 * (lane & 1);
    flgoff = (unsigned)(((frb * 16 + fcb) * 4 + fw) * 8);
  }
  unsigned aaddr[2];
#pragma unroll
  for (int k2 = 0; k2 < 2; k2++) aaddr[k2] = (wm * 64 + i16) * 128 + (((4 * k2 + g) ^ ((i16 >> 1) & 7)) * 16);
  const int fsw = ((i16 >> 3) & 1) | ((g & 1) << 1);
  unsigned bad[2][2];
#pragma unroll
  for (int nt = 0; nt < 2; nt++) {
    bad[nt][0] = BOFF + (8 * g + (i16 >> 2)) * 128 + (((wn * 2 + nt) ^ fsw) * 32) + (i16 & 3) * 8;
    bad[nt][1] = bad[nt][0] + 2 * BSTG;
  }
  asm volatile("" : "+v"(bad[0][0]), "+v"(bad[0][1]), "+v"(bad[1][0]), "+v"(bad[1][1]));
  const unsigned l16 = (unsigned)lane * 16u;
  char* const o_out = ws + WS_O + (size_t)(((rb * 32 + cb * 2 + wn) * 8 + wm * 4) * 1024);
  char* const kv_out = ws + WS_KVP + (size_t)((((cb * 2 + wn) * 2 + wm) * 16 + rb) * 2048);
  char* const z_out = ws + WS_Z + (size_t)(((cb * 2 + wn) * 16 + rb) * 128);
  f32x4 acc[4][6];
#pragma unroll
  for (int i = 0; i < 4; i++)
#pragma unroll
    for (int j = 0; j < 6; j++) acc[i][j] = f32x4{0, 0, 0, 0};
  bf16x8 fa[2][4];
  s16x4 fbl[2][6], fbh[2][6];
#define RDA6(buf, mt, st, k2) fa[buf][mt] = *(const LAS bf16x8*)(lds + aaddr[k2] + (st) * ASTG + (mt) * 2048)
#define BAD6(st, n, k2) (lds + ((st) == 2 ? bad[(n) & 1][1] : bad[(n) & 1][0] + (st) * BSTG) + ((n) >> 1) * 8192 + (k2) * 4096)
#define RDBL6(buf, n, st, k2) fbl[buf][n] = __builtin_amdgcn_ds_read_tr16_b64_v4i16((LAS s16x4*)(BAD6(st, n, k2)))
#define RDBH6(buf, n, st, k2) fbh[buf][n] = __builtin_amdgcn_ds_read_tr16_b64_v4i16((LAS s16x4*)(BAD6(st, n, k2) + 512))
#define FB6(buf, n) BC8(__builtin_shufflevector(fbl[buf][n], fbh[buf][n], 0, 1, 2, 3, 4, 5, 6, 7))
#define RD6(c, j, nst, nk2)                                               \
  if ((j) == 0) RDA6(c, 0, nst, nk2);                                     \
  else if ((j) <= 12) { if (((j) - 1) & 1) RDBH6(c, ((j) - 1) >> 1, nst, nk2); else RDBL6(c, ((j) - 1) >> 1, nst, nk2); } \
  else RDA6(c, (j) - 12, nst, nk2);
#define STEP6(c, RD, nst, nk2, BAR)                                                \
  _Pragma("unroll") for (int q = 0; q < 24; q++) {                                 \
    const int mt = q / 6, n = q % 6;                                               \
    if (q == 0 && (BAR)) { BARRIER(); SB(); }                                      \
    acc[mt][n] = n < 2 ? MFMA16(FB6(c, n), fa[c][mt], acc[mt][n]) : MFMA16(fa[c][mt], FB6(c, n), acc[mt][n]); \
    if (RD && q < 16) { RD6(c ^ 1, q, nst, nk2) }                                  \
    SB();                                                                          \
  }
#define TILE6(st, nst, LAST)            \
  STEP6(0, true, st, 1, false)          \
  STEP6(1, !(LAST), nst, 0, true)
#pragma unroll
  for (int i = 0; i < 4; i++)
    asm volatile("" : "+v"(acc[i][0]), "+v"(acc[i][1]), "+v"(acc[i][2]), "+v"(acc[i][3]), "+v"(acc[i][4]), "+v"(acc[i][5]));
  asm volatile("" : "+v"(flgoff), "+v"(aaddr[0]), "+v"(aaddr[1]));
  SB();
  BARRIER();
  STAMP(1);
#pragma unroll
  for (int j = 0; j < 16; j++) { RD6(0, j, 0, 0) }
  SB();
  u32x2 flg;
#pragma unroll 1
  for (int it = 0; it < 5; it++) {
    TILE6(0, 1, false) TILE6(1, 2, false) TILE6(2, 0, false)
    if (it == 1) {
      const u32x4 sv = {SENT, SENT, SENT, SENT};
      st16_sc1<0>(o_out, l16, sv); st16_sc1<1024>(o_out, l16, sv); st16_sc1<2048>(o_out, l16, sv); st16_sc1<3072>(o_out, l16, sv);
      st16_sc1<0>(kv_out, l16, sv); st16_sc1<1024>(kv_out, l16, sv);
      if (wm == 0 && lane < 8) st16_sc1<0>(z_out, l16, sv);
    }
    if (it == 3) {
      WAIT_VM(0);
      if (lane == 0) {
        const unsigned fo = (unsigned)(((rb * 16 + cb) * 4 + wid) * 8);
        asm volatile("global_store_dwordx2 %0, %1, %2 sc1" ::"v"(fo), "v"(tag), "s"(ws + WS_FLAG) : "memory");
      }
    }
    SB();
  }
  asm volatile("global_load_dwordx2 %0, %1, %2 sc1" : "=v"(flg) : "v"(flgoff), "s"(ws + WS_FLAG) : "memory");
  SB();
  TILE6(0, 1, true)
  SB();
  __builtin_amdgcn_s_setprio(2);
  STAMP(2);
  {
    f32x4 kv[2][2], zz[2];
#pragma unroll
    for (int a = 0; a < 2; a++) { kv[a][0] = f32x4{0, 0, 0, 0}; kv[a][1] = f32x4{0, 0, 0, 0}; zz[a] = f32x4{0, 0, 0, 0}; }
    const u32x4 ones = {0x3f803f80u, 0x3f803f80u, 0x3f803f80u, 0x3f803f80u};
#pragma unroll
    for (int p = 0; p < 2; p++) {
      u32x4 vf[2];
#pragma unroll
      for (int et = 0; et < 2; et++)
        vf[et] = u32x4{pack2(acc[2 * p][4 + et][0], acc[2 * p][4 + et][1]), pack2(acc[2 * p][4 + et][2], acc[2 * p][4 + et][3]),
                       pack2(acc[2 * p + 1][4 + et][0], acc[2 * p + 1][4 + et][1]), pack2(acc[2 * p + 1][4 + et][2], acc[2 * p + 1][4 + et][3])};
#pragma unroll
      for (int dt = 0; dt < 2; dt++) {
        u32x4 pk = {phi2(acc[2 * p][2 + dt][0], acc[2 * p][2 + dt][1]), phi2(acc[2 * p][2 + dt][2], acc[2 * p][2 + dt][3]),
                    phi2(acc[2 * p + 1][2 + dt][0], acc[2 * p + 1][2 + dt][1]), phi2(acc[2 * p + 1][2 + dt][2], acc[2 * p + 1][2 + dt][3])};
        kv[dt][0] = MFMA16(BC8(pk), BC8(vf[0]), kv[dt][0]);
        kv[dt][1] = MFMA16(BC8(pk), BC8(vf[1]), kv[dt][1]);
        zz[dt] = MFMA16(BC8(pk), BC8(ones), zz[dt]);
      }
    }
    f32x4 give0 = wm ? kv[0][0] : kv[0][1], give1 = wm ? kv[1][0] : kv[1][1];
    f32x4 keep0 = wm ? kv[0][1] : kv[0][0], keep1 = wm ? kv[1][1] : kv[1][0];
    *(LAS f32x4*)(lds + XCH + wid * 2048 + l16) = give0;
    *(LAS f32x4*)(lds + XCH + wid * 2048 + 1024 + l16) = give1;
    if (wm == 1 && i16 == 0) {
      *(LAS f32x4*)(lds + MISC + 128 + wn * 128 + g * 32) = zz[0];
      *(LAS f32x4*)(lds + MISC + 128 + wn * 128 + g * 32 + 16) = zz[1];
    }
    asm volatile("" ::: "memory");
    if (lane == 0) lflag[wid] = 1u;
    lds_wait(lflag + (wid ^ 1));
    asm volatile("" ::: "memory");
    keep0 += *(const LAS f32x4*)(lds + XCH + (wid ^ 1) * 2048 + l16);
    keep1 += *(const LAS f32x4*)(lds + XCH + (wid ^ 1) * 2048 + 1024 + l16);
    u32x4 s0 = __builtin_bit_cast(u32x4, keep0), s1 = __builtin_bit_cast(u32x4, keep1);
#pragma unroll
    for (int j = 0; j < 4; j++) { s0[j] = min(s0[j], SENT - 1); s1[j] = min(s1[j], SENT - 1); }
    st16_sc1<0>(kv_out, l16, s0);
    st16_sc1<1024>(kv_out, l16, s1);
    if (wm == 0 && i16 == 0) {
      f32x4 z0 = zz[0] + *(const LAS f32x4*)(lds + MISC + 128 + wn * 128 + g * 32);
      f32x4 z1 = zz[1] + *(const LAS f32x4*)(lds + MISC + 128 + wn * 128 + g * 32 + 16);
      u32x4 t0 = __builtin_bit_cast(u32x4, z0), t1 = __builtin_bit_cast(u32x4, z1);
#pragma unroll
      for (int j = 0; j < 4; j++) { t0[j] = min(t0[j], SENT - 1); t1[j] = min(t1[j], SENT - 1); }
      st16_sc1<0>(z_out, (unsigned)g * 32u, t0);
      st16_sc1<16>(z_out, (unsigned)g * 32u, t1);
    }
  }
  SB();
  STAMP(3);
  u32x4 pq[4];
#pragma unroll
  for (int mt = 0; mt < 4; mt++)
    pq[mt] = u32x4{phi2(acc[mt][0][0], acc[mt][0][1]), phi2(acc[mt][0][2], acc[mt][0][3]),
                   phi2(acc[mt][1][0], acc[mt][1][1]), phi2(acc[mt][1][2], acc[mt][1][3])};
  asm volatile("" : "+v"(pq[0]), "+v"(pq[1]), "+v"(pq[2]), "+v"(pq[3]));
  SB();
  {
    asm volatile("s_waitcnt vmcnt(0)" : "+v"(flg)::"memory");
    int spins = 0;
    while (__builtin_amdgcn_ballot_w64(flg[0] != tag[0] || flg[1] != tag[1]) != 0ull) {
      if (++spins > SPIN_CAP) break;
      __builtin_amdgcn_s_sleep(2);
      asm volatile("global_load_dwordx2 %0, %1, %2 sc1\n\ts_waitcnt vmcnt(0)" : "=v"(flg) : "v"(flgoff), "s"(ws + WS_FLAG) : "memory");
    }
  }
  u32x4 kvf[2], zf;
  {
    const char* kv_in = ws + WS_KVP + (size_t)(((cb * 2 + wn) * 2 + wm) * 16) * 2048;
    const char* z_in = ws + WS_Z + (size_t)((cb * 2 + wn) * 16) * 128;
    const unsigned zo = (unsigned)(i16 * 128 + g * 32);
    f32x4 sk0, sk1, sz0, sz1;
    int spins = 0;
    while (true) {
      u32x4 pv[32], zv[2];
      ld16_sc1<0>(zv[0], z_in, zo); ld16_sc1<16>(zv[1], z_in, zo);
#pragma unroll
      for (int q = 0; q < 8; q++) {
        ld16_sc1<0>(pv[4 * q], kv_in + q * 4096, l16); ld16_sc1<1024>(pv[4 * q + 1], kv_in + q * 4096, l16);
        ld16_sc1<2048>(pv[4 * q + 2], kv_in + q * 4096, l16); ld16_sc1<3072>(pv[4 * q + 3], kv_in + q * 4096, l16);
      }
      asm volatile("s_waitcnt vmcnt(0)"
                   : "+v"(zv[0]), "+v"(zv[1]), "+v"(pv[0]), "+v"(pv[1]), "+v"(pv[2]), "+v"(pv[3]), "+v"(pv[4]), "+v"(pv[5]), "+v"(pv[6]), "+v"(pv[7]),
                     "+v"(pv[8]), "+v"(pv[9]), "+v"(pv[10]), "+v"(pv[11]), "+v"(pv[12]), "+v"(pv[13]), "+v"(pv[14]), "+v"(pv[15])::"memory");
      asm volatile(""
                   : "+v"(pv[16]), "+v"(pv[17]), "+v"(pv[18]), "+v"(pv[19]), "+v"(pv[20]), "+v"(pv[21]), "+v"(pv[22]), "+v"(pv[23]),
                     "+v"(pv[24]), "+v"(pv[25]), "+v"(pv[26]), "+v"(pv[27]), "+v"(pv[28]), "+v"(pv[29]), "+v"(pv[30]), "+v"(pv[31])::"memory");
      unsigned mx = 0;
#pragma unroll
      for (int q = 0; q < 32; q++) mx = max(max(mx, max(pv[q][0], pv[q][1])), max(pv[q][2], pv[q][3]));
      mx = max(max(mx, max(zv[0][0], zv[0][1])), max(zv[0][2], zv[0][3]));
      mx = max(max(mx, max(zv[1][0], zv[1][1])), max(zv[1][2], zv[1][3]));
      sk0 = __builtin_bit_cast(f32x4, pv[0]); sk1 = __builtin_bit_cast(f32x4, pv[1]);
#pragma unroll
      for (int q = 1; q < 16; q++) { sk0 += __builtin_bit_cast(f32x4, pv[2 * q]); sk1 += __builtin_bit_cast(f32x4, pv[2 * q + 1]); }
      sz0 = __builtin_bit_cast(f32x4, zv[0]); sz1 = __builtin_bit_cast(f32x4, zv[1]);
      if (__builtin_amdgcn_ballot_w64(mx == SENT) == 0ull) break;
      if (++spins > (SPIN_CAP >> 4)) break;
      __builtin_amdgcn_s_sleep(4);
    }
    STAMP(4);
    u32x4 mine = {pack2(sk0[0], sk0[1]), pack2(sk0[2], sk0[3]), pack2(sk1[0], sk1[1]), pack2(sk1[2], sk1[3])};
    *(LAS u32x4*)(lds + XCH + (wid ^ 1) * 2048 + l16) = mine;
    asm volatile("" ::: "memory");
    if (lane == 0) lflag[4 + wid] = 1u;
#pragma unroll
    for (int s = 0; s < 4; s++)
#pragma unroll
      for (int j = 0; j < 4; j++) { sz0[j] = dppadd(sz0[j], s); sz1[j] = dppadd(sz1[j], s); }
    zf = u32x4{pack2(sz0[0], sz0[1]), pack2(sz0[2], sz0[3]), pack2(sz1[0], sz1[1]), pack2(sz1[2], sz1[3])};
    lds_wait(lflag + 4 + (wid ^ 1));
    asm volatile("" ::: "memory");
    u32x4 other = *(const LAS u32x4*)(lds + XCH + wid * 2048 + l16);
    kvf[0] = wm ? other : mine;
    kvf[1] = wm ? mine : other;
  }
#pragma unroll
  for (int mt = 0; mt < 4; mt++) {
    const f32x4 zero = {0, 0, 0, 0};
    f32x4 den = MFMA16(BC8(zf), BC8(pq[mt]), zero);
    f32x4 n0 = MFMA16(BC8(kvf[0]), BC8(pq[mt]), zero);
    f32x4 n1 = MFMA16(BC8(kvf[1]), BC8(pq[mt]), zero);
    float rd = __builtin_amdgcn_rcpf(rbf(rbf(den[0]) + 1e-6f));
    unsigned t[4] = {pack2(n0[0], n0[1]), pack2(n0[2], n0[3]), pack2(n1[0], n1[1]), pack2(n1[2], n1[3])};
    u32x4 ov;
#pragma unroll
    for (int j = 0; j < 4; j++) ov[j] = min(pack2(lo2f(t[j]) * rd, hi2f(t[j]) * rd), SENT - 1);
    if (mt == 0) st16_sc1<0>(o_out, l16, ov);
    if (mt == 1) st16_sc1<1024>(o_out, l16, ov);
    if (mt == 2) st16_sc1<2048>(o_out, l16, ov);
    if (mt == 3) st16_sc1<3072>(o_out, l16, ov);
  }
  SB();
  STAMP(5);
  {
    const int kh = wn;
    const char* o_in = ws + WS_O + (size_t)(((rb2 * 32 + kh * 16) * 8 + wm * 4) * 1024);
    lds_wait(lflag + 8); lds_wait(lflag + 9); lds_wait(lflag + 10); lds_wait(lflag + 11);
    const int q4 = i16 >> 2, p4 = i16 & 3;
    const int sw = (2 * g + (q4 >> 1)) & 3;
    unsigned wad[4];
#pragma unroll
    for (int nt = 0; nt < 4; nt++) wad[nt] = (unsigned)(kh * 14 * 4096 + (4 * g + q4) * 128 + ((nt ^ sw) * 32) + p4 * 8);
    const int late = SS + 2 * kh * SCH - kh * 14 * 4096;
    f32x4 a2[4][4];
    int tries = 0;
    while (true) {
      {
        const unsigned po = (unsigned)(i16 * 8192 + 3072 + g * 256);
        int spins = 0;
        while (true) {
          unsigned pr;
          asm volatile("global_load_dword %0, %1, %2 sc1\n\ts_waitcnt vmcnt(0)" : "=v"(pr) : "v"(po), "s"(o_in) : "memory");
          if (__builtin_amdgcn_ballot_w64(pr == SENT) == 0ull) break;
          if (++spins > SPIN_CAP) break;
          __builtin_amdgcn_s_sleep(2);
        }
      }
      STAMP(6);
      u32x4 of[8][4];
#define OLOAD(i)                                                                    \
  ld16_sc1<0>(of[(i) & 7][0], o_in + (i) * 8192, l16); ld16_sc1<1024>(of[(i) & 7][1], o_in + (i) * 8192, l16); \
  ld16_sc1<2048>(of[(i) & 7][2], o_in + (i) * 8192, l16); ld16_sc1<3072>(of[(i) & 7][3], o_in + (i) * 8192, l16);
#pragma unroll
      for (int i = 0; i < 8; i++) { OLOAD(i) }
#pragma unroll
      for (int a = 0; a < 4; a++)
#pragma unroll
        for (int b = 0; b < 4; b++) a2[a][b] = f32x4{0, 0, 0, 0};
      unsigned mx = 0;
      s16x4 wl[2][4], wh[2][4];
#define WOFF(i) ((i) < 14 ? (i) * 4096 : late + ((i) - 14) * SCH)
#define WLOAD(i)                                                                    \
  _Pragma("unroll") for (int nt = 0; nt < 4; nt++) {                                \
    wl[(i) & 1][nt] = __builtin_amdgcn_ds_read_tr16_b64_v4i16((LAS s16x4*)(lds + wad[nt] + WOFF(i)));        \
    wh[(i) & 1][nt] = __builtin_amdgcn_ds_read_tr16_b64_v4i16((LAS s16x4*)(lds + wad[nt] + WOFF(i) + 2048)); \
  }
      WLOAD(0)
#pragma unroll
      for (int i = 0; i < 16; i++) {
        SB();
        if (i == 13) { lds_wait(lflag + 12 + 2 * kh); lds_wait(lflag + 13 + 2 * kh); asm volatile("" ::: "memory"); }
        if (i < 15) { WLOAD(i + 1) }
        asm volatile("s_waitcnt vmcnt(%4)" : "+v"(of[i & 7][0]), "+v"(of[i & 7][1]), "+v"(of[i & 7][2]), "+v"(of[i & 7][3]) : "n"(i < 8 ? 28 : 4 * (15 - i)) : "memory");
        SB();
#pragma unroll
        for (int rt = 0; rt < 4; rt++) {
          mx = max(max(mx, max(of[i & 7][rt][0], of[i & 7][rt][1])), max(of[i & 7][rt][2], of[i & 7][rt][3]));
#pragma unroll
          for (int nt = 0; nt < 4; nt++)
            a2[rt][nt] = MFMA16(BC8(__builtin_shufflevector(wl[i & 1][nt], wh[i & 1][nt], 0, 1, 2, 3, 4, 5, 6, 7)), BC8(of[i & 7][rt]), a2[rt][nt]);
        }
        asm volatile("" : "+v"(mx));
        SB();
        if (i < 8) { OLOAD(i + 8) }
      }
      if (__builtin_amdgcn_ballot_w64(mx == SENT) == 0ull) break;
      if (++tries > 64) break;
    }
    STAMP(7);
    BARRIER();
    f32x4 keep[4][2];
#pragma unroll
    for (int rt = 0; rt < 4; rt++)
#pragma unroll
      for (int e = 0; e < 2; e++) {
        f32x4 gv;
#pragma unroll
        for (int j = 0; j < 4; j++) {
          const float lo = a2[rt][e][j], hi = a2[rt][2 + e][j];
          gv[j] = kh ? lo : hi;
          keep[rt][e][j] = kh ? hi : lo;
        }
        *(LAS f32x4*)(lds + wid * 8192 + (rt * 2 + e) * 1024 + l16) = gv;
      }
    BARRIER();
    if (bid == 0 && tid == 0)
      asm volatile("global_store_dwordx2 %0, %1, %2 sc1" ::"v"(0u), "v"(tag), "s"(ws + WS_EPOCH) : "memory");
    u16* orow = out + (size_t)(rb2 * 128 + wm * 64 + i16) * 2048 + cb2 * 64 + kh * 32 + 4 * g;
#pragma unroll
    for (int rt = 0; rt < 4; rt++)
#pragma unroll
      for (int e = 0; e < 2; e++) {
        f32x4 o = *(const LAS f32x4*)(lds + (wid ^ 2) * 8192 + (rt * 2 + e) * 1024 + l16);
        o += keep[rt][e];
        *(u32x2*)(orow + (size_t)rt * 16 * 2048 + e * 16) = u32x2{pack2(o[0], o[1]), pack2(o[2], o[3])};
      }
  }
}

extern "C" void kernel_launch(void* const* d_in, const int* in_sizes, int n_in, void* d_out, int out_size,
                              void* d_ws, size_t ws_size, hipStream_t stream) {
  (void)hipFuncSetAttribute((const void*)fused, hipFuncAttributeMaxDynamicSharedMemorySize, LDSZ);
  fused<<<256, 512, LDSZ, stream>>>((const u16*)d_in[0], (const u16*)d_in[1], (const u16*)d_in[2], (const u16*)d_in[3],
                                    (const u16*)d_in[4], (const u16*)d_in[5], (const u16*)d_in[6], (const u16*)d_in[7],
                                    (const u16*)d_in[8], (const u16*)d_in[9], (u16*)d_out, (char*)d_ws);
}
```

```cpp
#define KS 0
#define DYN 2
#define P2A 99
#define ROT 8
#define G2P 0
#define CPB 1
#define UL 0
#define YSK 0
#define YP 0
#define YL 2
#define YR 10
#define QE 4
#define QR0 4
#define G2PD 3
#define RWN 10
#define PH0 3
#define WPH 0
#define WN2 8
#define WO_P
#define WO_R XFREE WOL2(0, 5) WOL2(1, 5) WOL2(0, 6) WOL2(1, 6) WOL2(0, 7) WOL2(1, 7) WOL2(0, 8) WOL2(1, 8) OPOLL WOL2(0, 9) WOL2(1, 9) WOL2(0, 10) WOL2(1, 10) WOL2(0, 11) WOL2(1, 11) WOL2(0, 14) WOL2(1, 14)
#define CEV(t, h, q) if ((t) == 1 && !(h) && (q) == 20) { TAG1 } \
  if ((t) == 1 && !(h) && (q) == 21) { TAG2 } \
  if ((t) == 1 && !(h) && (q) == 22) { TAG3 } \
  if ((t) == 5 && !(h) && (q) == 20) { CLRA CLRK(0) } \
  if ((t) == 5 && !(h) && (q) == 21) { CLRK(1) } \
  if ((t) == 6 && !(h) && (q) == 20) { CLRA CLRO(0) } \
  if ((t) == 6 && !(h) && (q) == 21) { CLRO(1) } \
  if ((t) == 6 && !(h) && (q) == 22) { CLRO(2) } \
  if ((t) == 6 && !(h) && (q) == 23) { CLRO(3) } \
  if ((t) == 8 && !(h) && (q) == 20) { EXP0 } \
  if ((t) == 19 && !(h) && (q) == 15) { QFL(48) } \
  if ((t) == 20 && !(h) && (q) == 15) { QFL(52) }
#define CPRE(t) if ((t) == 0) WAIT_VM(0); if ((t) == 16) WAIT_VM(4);
#include <hip/hip_runtime.h>
#include <hip/hip_bf16.h>
#include <stdint.h>

#define WOW 0
#define XW 8
#define GS1 1
#define G1 8
#define VW 1
#define P3A 99
#define UNT " nt"
#define PNT " nt"
#define ONT 0
#define CNT 0
#define SNT 0
#define GNT
#define XNT 0
#define WNT 0
#define TNT 0
#define YPR 1
#define YQ 7
#define G2R 99
#define YPL 0
#define NB 6
#define HWO 0
#define NSQ 6
#define F16 1
#define NF 10
#define XS0 1
#define PFS 1
#define NPA 0
#define HPRIO 1
typedef unsigned short u16;
typedef unsigned long long u64;
typedef __attribute__((ext_vector_type(8))) __bf16 bf16x8;
typedef __attribute__((ext_vector_type(2))) __bf16 bf16x2;
typedef __attribute__((ext_vector_type(4))) short s16x4;
typedef __attribute__((ext_vector_type(4))) float f32x4;
typedef __attribute__((ext_vector_type(2))) float f32x2;
typedef __attribute__((ext_vector_type(4))) unsigned u32x4;
typedef __attribute__((ext_vector_type(2))) unsigned u32x2;
#define LAS __attribute__((address_space(3)))
#if defined(__HIP_DEVICE_COMPILE__)
#define NOPK __attribute__((target("no-packed-fp32-ops")))
#else
#define NOPK
#endif
#define DI __device__ __forceinline__ NOPK

#define MFMA16(a, b, c) __builtin_amdgcn_mfma_f32_16x16x32_bf16((a), (b), (c), 0, 0, 0)
#define BARRIER() asm volatile("s_waitcnt lgkmcnt(0)\n\ts_barrier" ::: "memory")
#define WAIT_VM(n) asm volatile("s_waitcnt vmcnt(%0)" ::"n"(n) : "memory")
#define SB() __builtin_amdgcn_sched_barrier(0)
#define BC8(x) __builtin_bit_cast(bf16x8, (x))

DI unsigned pack2(float a, float b) {
  f32x2 v = {a, b};
  return __builtin_bit_cast(unsigned, __builtin_convertvector(v, bf16x2));
}
DI unsigned vv(unsigned a) { asm volatile("" : "+v"(a)); return a; }
DI float vf1(float a) { asm volatile("" : "+v"(a)); return a; }
DI u32x2 vv2(unsigned a, unsigned b) { return u32x2{vv(a), vv(b)}; }
DI u32x4 vv4(unsigned a) { return u32x4{vv(a), vv(a), vv(a), vv(a)}; }
#define TAGV vv2(tag[0], tag[1])
DI float lo2f(unsigned p) { return __builtin_bit_cast(float, p << 16); }
DI float hi2f(unsigned p) { return __builtin_bit_cast(float, p & 0xffff0000u); }
DI float rbf(float x) { return lo2f(pack2(x, x)); }
DI float phi1(float s) {
  return __builtin_amdgcn_fmed3f(__builtin_amdgcn_exp2f(__builtin_fmaf(s, 1.44269504f, -1.44269504e-6f)), 0.f, 1.f) + __builtin_elementwise_maximum(s, 1e-6f);
}
DI unsigned phi2(float x, float y) { return pack2(phi1(x), phi1(y)); }
DI void glds16nt(const char* base, unsigned voff, unsigned ldsaddr) {
  asm volatile("s_mov_b32 m0, %0\n\ts_nop 0\n\tglobal_load_lds_dwordx4 %1, %2 nt" ::"s"(ldsaddr), "v"(voff), "s"(base) : "memory");
}
DI void glds16(const char* base, unsigned voff, unsigned ldsaddr) {
  asm volatile("s_mov_b32 m0, %0\n\ts_nop 0\n\tglobal_load_lds_dwordx4 %1, %2" ::"s"(ldsaddr), "v"(voff), "s"(base) : "memory");
}
template <int OFF, int NT = 0> DI void gldso(const char* base, unsigned voff, unsigned ldsbase) {
  if (NT) asm volatile("s_add_i32 m0, %0, %3\n\ts_nop 0\n\tglobal_load_lds_dwordx4 %1, %2 nt" ::"s"(ldsbase), "v"(voff), "s"(base), "n"(OFF) : "memory", "scc");
  else asm volatile("s_add_i32 m0, %0, %3\n\ts_nop 0\n\tglobal_load_lds_dwordx4 %1, %2" ::"s"(ldsbase), "v"(voff), "s"(base), "n"(OFF) : "memory", "scc");
}
DI void gldsn(const char* base, unsigned voff, unsigned ldsbase, const int off) {
  asm volatile("s_add_i32 m0, %0, %3\n\ts_nop 0\n\tglobal_load_lds_dwordx4 %1, %2" ::"s"(ldsbase), "v"(voff), "s"(base), "n"(off) : "memory", "scc");
}
DI void glds16sc1(const char* base, unsigned voff, unsigned ldsaddr) {
  asm volatile("s_mov_b32 m0, %0\n\ts_nop 0\n\tglobal_load_lds_dwordx4 %1, %2 sc1" GNT ::"s"(ldsaddr), "v"(voff), "s"(base) : "memory");
}
template <int OFF, int NT = 0> DI void st16_sc1(char* base, unsigned voff, u32x4 v) {
  if (NT == 1) asm volatile("global_store_dwordx4 %0, %1, %2 offset:%3 sc1 nt\n\ts_nop 1" ::"v"(voff), "v"(v), "s"(base), "n"(OFF) : "memory");
  else if (NT == 2) asm volatile("global_store_dwordx4 %0, %1, %2 offset:%3 nt\n\ts_nop 1" ::"v"(voff), "v"(v), "s"(base), "n"(OFF) : "memory");
  else if (NT == 3) asm volatile("global_store_dwordx4 %0, %1, %2 offset:%3 sc0 sc1\n\ts_nop 1" ::"v"(voff), "v"(v), "s"(base), "n"(OFF) : "memory");
  else asm volatile("global_store_dwordx4 %0, %1, %2 offset:%3 sc1\n\ts_nop 1" ::"v"(voff), "v"(v), "s"(base), "n"(OFF) : "memory");
}
template <int OFF> DI void ld16_sc1(u32x4& d, const char* base, unsigned voff) {
  asm volatile("global_load_dwordx4 %0, %1, %2 offset:%3 sc1" : "=v"(d) : "v"(voff), "s"(base), "n"(OFF) : "memory");
}
DI float dppadd(float v, const int ctrl_sel) {
  int iv = __builtin_bit_cast(int, v), r;
  if (ctrl_sel == 0) r = __builtin_amdgcn_update_dpp(0, iv, 0xB1, 0xf, 0xf, true);
  else if (ctrl_sel == 1) r = __builtin_amdgcn_update_dpp(0, iv, 0x4E, 0xf, 0xf, true);
  else if (ctrl_sel == 2) r = __builtin_amdgcn_update_dpp(0, iv, 0x141, 0xf, 0xf, true);
  else r = __builtin_amdgcn_update_dpp(0, iv, 0x140, 0xf, 0xf, true);
  return v + __builtin_bit_cast(float, r);
}

constexpr size_t WS_EPOCH = 0;
constexpr size_t WS_FLAG = 131072;
constexpr size_t WS_FLAG2 = 196608;
constexpr size_t WS_YF = 262144;
constexpr size_t WS_H1 = 524288;
constexpr size_t WS_H2 = 655360;
constexpr size_t WS_Z = 65536;
constexpr size_t WS_KVP = 1u << 20;
constexpr size_t WS_O = 4u << 20;
constexpr size_t WS_UT = 8u << 20;
constexpr size_t WS_YT = 14u << 20;
constexpr int UP1 = 4096 + UL * 256, UP2 = 4 * UP1, UP3 = 16 * UP2 + UL * 1024, YRP = 262144 + YSK;
constexpr unsigned SENT = 0xFFFFFFFFu;
constexpr int SPIN_CAP = 1 << 17;

constexpr int STG = 16384, WOFF = 3 * STG;
constexpr int SS = 6 * STG, SCH = 8704;
constexpr int MISC = SS + 4 * SCH;
constexpr int QF = MISC + 1024;
constexpr int XCH = QF;
constexpr int UB = QF + 8192;
constexpr int LDSZ = 163840;
constexpr int TP = 528;
constexpr int KH = 12 * 4096;
static_assert(UB + 4 * 4608 <= LDSZ, "lds");
#define wo_slot(kh, i)                                                                        \
  ((i) == 15 ? SS + 2 * (kh) * SCH : (i) == 14 ? XCH + (kh) * 4096 : (i) >= 12 ? SS + (2 * (kh) + (i) - 12) * SCH + 4608 : (12 * (kh) + (i)) * 4096)

#define STAMP(i)

#define KSTAMP(i)
#define PROBE(k)
DI void lds_wait(volatile LAS unsigned* f) {
  int spins = 0;
  while (*f == 0u) {
    __builtin_amdgcn_s_sleep(1);
    if (++spins > SPIN_CAP) break;
  }
}
DI void lds_wait4(volatile LAS unsigned* f) {
  int spins = 0;
  while (true) {
    u32x4 v = *(volatile LAS u32x4*)f;
    if (__builtin_amdgcn_readfirstlane(v[0] & v[1] & v[2] & v[3]) != 0) break;
    __builtin_amdgcn_s_sleep(1);
    if (++spins > SPIN_CAP) break;
  }
}
DI void lds_wait_ge(unsigned addr, const int off, const int val, unsigned skip) {
  unsigned t, r, n;
  asm volatile("s_cmp_lg_u32 %6, 0\n\ts_cbranch_scc1 2f\n\ts_mov_b32 %2, 0x20000\n"
               "1:\n\tds_read_b32 %0, %3 offset:%4\n\ts_waitcnt lgkmcnt(0)\n\tv_readfirstlane_b32 %1, %0\n\t"
               "s_cmp_ge_u32 %1, %5\n\ts_cbranch_scc1 2f\n\ts_sub_u32 %2, %2, 1\n\ts_cbranch_scc0 1b\n2:"
               : "=&v"(t), "=&s"(r), "=&s"(n) : "v"(addr), "n"(off), "n"(val), "s"(skip) : "memory", "scc");
}
DI void lds_wait_all(unsigned addr, const int off) {
  unsigned t, n;
  asm volatile("s_mov_b32 %1, 0x20000\n"
               "1:\n\tds_read_b32 %0, %2 offset:%3\n\ts_waitcnt lgkmcnt(0)\n\tv_cmp_eq_u32 vcc, 0, %0\n\t"
               "s_cbranch_vccz 2f\n\ts_sub_u32 %1, %1, 1\n\ts_cbranch_scc0 1b\n2:"
               : "=&v"(t), "=&s"(n) : "v"(addr), "n"(off) : "memory", "scc", "vcc");
}
DI void lds_get16(u32x4& d, unsigned f, unsigned a) {
  unsigned t, n;
  asm volatile("s_mov_b32 %2, 0x20000\n"
               "1:\n\tds_read_b32 %1, %3\n\tds_read_b128 %0, %4\n\ts_waitcnt lgkmcnt(0)\n\tv_cmp_eq_u32 vcc, 0, %1\n\t"
               "s_cbranch_vccz 2f\n\ts_sub_u32 %2, %2, 1\n\ts_cbranch_scc0 1b\n2:"
               : "=&v"(d), "=&v"(t), "=&s"(n) : "v"(f), "v"(a) : "memory", "scc", "vcc");
}
typedef u32x2 __attribute__((aligned(4))) u32x2a;
#define CBX 0
DI int fresh_lane() {
  unsigned z = 0;
  asm volatile("" : "+v"(z));
  return (int)__builtin_amdgcn_mbcnt_hi(~0u, __builtin_amdgcn_mbcnt_lo(~0u, z));
}
#define RELANE const int lane = fresh_lane(), i16 = lane & 15, g = lane >> 4; const unsigned l16 = (unsigned)lane * 16u; (void)i16; (void)g; (void)l16;
#define RELANE2 RELANE const int r8 = lane >> 3, cp = lane & 7; \
  const unsigned wo_e = (unsigned)((16 * (r8 >> 2) + (r8 & 3)) * 2048 + cb2 * 128 + ((cp ^ (((r8 >> 1) & 1) << 1)) * 16)), wo_o = wo_e ^ 64u; (void)wo_o;
DI int cg_of(int rb_, int cb_) { cb_ ^= CBX; return ((cb_ >> 2) * 2 + (rb_ >> 3)) * 32 + (cb_ & 3) * 8 + (rb_ & 7); }
DI unsigned max4(u32x4 v) { return max(max(v[0], v[1]), max(v[2], v[3])); }
DI float dot2(unsigned a, unsigned b, float c) {
  return __builtin_amdgcn_fdot2_f32_bf16(__builtin_bit_cast(bf16x2, a), __builtin_bit_cast(bf16x2, b), c, false);
}

__global__ __launch_bounds__(512, 2) NOPK void fused(const u16* __restrict__ x, const u16* __restrict__ w,
                                                     const u16* __restrict__ wo, const u16* __restrict__ u,
                                                     const u16* __restrict__ lar, const u16* __restrict__ aim,
                                                     const u16* __restrict__ cre, const u16* __restrict__ cim,
                                                     const u16* __restrict__ ldt, const u16* __restrict__ dsk,
                                                     u16* __restrict__ out, char* __restrict__ ws) {
  asm volatile(".macro pkrn s, r\n.set \\s, 0\n.set pk_s, 0\n.irpc c, \"\\r\"\n.ifc \\c,:\n.set pk_s, 2\n.endif\n.if pk_s == 1\n.set \\s, \\s*10+\\c\n.endif\n"
               ".ifc \\c,[\n.set pk_s, 1\n.endif\n.endr\n.endm\n"
               ".macro pkadv o0, o1, p, t, a0, a1, b0, b1\n"
               "pkrn pk_o0, \"\\o0\"\npkrn pk_o1, \"\\o1\"\npkrn pk_p, \"\\p\"\npkrn pk_t, \"\\t\"\n"
               "pkrn pk_a0, \"\\a0\"\npkrn pk_a1, \"\\a1\"\npkrn pk_b0, \"\\b0\"\npkrn pk_b1, \"\\b1\"\n"
               ".long 0xd3b14000 + pk_p, 0x08020100 + pk_a0 + pk_b0 * 512\n"
               ".long 0xd3b14000 + pk_t, 0x08020100 + pk_a1 + pk_b1 * 512\n"
               ".long 0xd3b05800 + pk_o0, 0x34020100 + pk_a0 + pk_b0 * 512 + pk_p * 262144\n"
               ".long 0xd3b05800 + pk_o1, 0x34020100 + pk_a1 + pk_b1 * 512 + pk_t * 262144\n"
               "v_cvt_pk_bf16_f32 v[pk_p], v[pk_o0], v[pk_o0+1]\n"
               "v_cvt_pk_bf16_f32 v[pk_p+1], v[pk_o1], v[pk_o1+1]\n.endm");
  extern __shared__ __attribute__((aligned(16))) char smem[];
  LAS char* lds = (LAS char*)smem;
  const int tid = threadIdx.x, lane = tid & 63;
  const int wid = __builtin_amdgcn_readfirstlane(tid >> 6);
  const int bid = blockIdx.x;
  const int xcd = bid & 7, idx = bid >> 3;
  const int rb = (xcd & 1) * 8 + (idx & 7);
  const int cb = ((xcd >> 1) * 4 + (idx >> 3)) ^ CBX;
  const int rb2 = (xcd & 3) * 4 + (idx & 3);
  const int cb2 = (xcd >> 2) * 8 + (idx >> 2);
  const int cgid = xcd * 32 + idx;
  const int rot = (ROT * (idx >> 2)) & 15;
  const int i16 = lane & 15, g = lane >> 4;
  const unsigned l16 = (unsigned)lane * 16u;
  volatile LAS unsigned* lflag = (volatile LAS unsigned*)(lds + MISC);
  STAMP(0);
  PROBE(0)
  u64 pc0;
  int clen, clen2;
  asm volatile("s_getpc_b64 %0\n.Lkbeg:\n\ts_mov_b32 %1, .Lkend-.Lkbeg\n\ts_mov_b32 %2, .Lkend2-.Lkbeg" : "=s"(pc0), "=s"(clen), "=s"(clen2));
  clen = max(clen, clen2);
  const int wm = wid & 1, wn = (wid >> 1) & 1;
  char* const o_out = ws + WS_O + (size_t)((rb * 32 + cb * 2 + wn) * 8192);
  char* const kv_out = ws + WS_KVP + (size_t)((((cb * 2 + wn) * 2 + wm) * 16 + rb) * 1024);
  char* const z_out = ws + WS_Z + (size_t)(((cb * 2 + wn) * 16 + rb) * 128);
  const unsigned w1k = (unsigned)(wid & 3) * 1024u;
#define WOX const unsigned wox = (wo_e ^ (unsigned)((wid & 1) * 64)) + (unsigned)(wid & 3) * 8192u;
#define WOLD(kh, i)                                                                          \
  {                                                                                          \
    int ri = rot;                                                                            \
    asm volatile("" : "+s"(ri));                                                             \
    gldso<wo_slot(kh, i)>((const char*)wo + ((kh) * 16 + (((i) + ri) & 15)) * 65536, wox, w1k); \
  }
#define WOXR unsigned woxr; { RELANE2 WOX woxr = wox | (unsigned)rot << 16; asm volatile("" : "+v"(woxr)); }
#define WOL2(kh, i)                                                                          \
  {                                                                                          \
    unsigned t_;                                                                             \
    asm volatile("s_add_i32 m0, %1, %4\n\tv_xor_b32 %0, %5, %2\n\tglobal_load_lds_dwordx4 %0, %3"         \
                 : "=&v"(t_) : "s"(w1k), "v"(woxr), "s"(wo), "n"(wo_slot(kh, i)), "n"(((kh) * 16 + (i)) << 16) : "memory", "scc"); \
  }
#define HINT(region) asm volatile("s_mov_b64 exec, 1\n\tglobal_store_dwordx2 %0, %1, %2 sc1\n\ts_mov_b64 exec, -1" \
                                  ::"v"((unsigned)((cgid * 4 + wid) * 128)), "v"(TAGV), "s"(ws + (region)) : "memory");
#define QFL(i) asm volatile("s_mov_b64 exec, 1\n\tds_write_b32 %0, %1 offset:%2\n\ts_mov_b64 exec, -1" ::"v"((unsigned)(MISC + wid * 4)), "v"(1u), "n"((i) * 4) : "memory");
#define FLAGBAD (__builtin_amdgcn_ballot_w64(flg[0] != tag[0] || flg[1] != tag[1]) != 0ull)
  u64 ep;
  u32x2 tag;
#define TAG1 asm volatile("" : "+s"(ep)); ep += 0x9E3779B97F4A7C15ull; ep = (ep ^ (ep >> 30)) * 0xBF58476D1CE4E5B9ull; asm volatile("" : "+s"(ep));
#define TAG2 ep = (ep ^ (ep >> 27)) * 0x94D049BB133111EBull; asm volatile("" : "+s"(ep));
#define TAG3 ep ^= ep >> 31; asm volatile("" : "+s"(ep)); tag = u32x2{(unsigned)ep, (unsigned)(ep >> 32)};

  const int lw = wid & 3;
  const int r_ = idx & 7, c_ = idx >> 3;
  const int koff = KS == 0 ? 4 * ((r_ & 1) + 2 * (((r_ >> 1) ^ c_) & 1)) + (xcd & 3)
                 : KS == 1 ? 0
                 : KS == 4 ? 4 * ((r_ & 1) + 2 * (((r_ >> 1) ^ c_) & 1))
                 : KS == 7 ? 8 * ((r_ ^ c_) & 1) + 2 * (xcd & 3)
                 : KS == 10 ? (2 * r_ + 4 * c_) & 15
                 : 2 * (xcd & 7);
  const unsigned la0 = lw * 4096, lq0 = WOFF + lw * 2048;
#define TADR(k0)                                                                                  \
  unsigned aoff[4], boff[4], qoff[2];                                                             \
  {                                                                                               \
    RELANE                                                                                        \
    const int r8 = lane >> 3, cp = lane & 7;                                                      \
    _Pragma("unroll") for (int ii = 0; ii < 4; ii++) {                                            \
      int row = (4 * lw + ii) * 8 + r8;                                                           \
      int c = cp ^ ((row >> 1) & 7);                                                              \
      aoff[ii] = (unsigned)((rb * 128 + row) * 2048 + c * 16);                                    \
    }                                                                                             \
    _Pragma("unroll") for (int jj = 0; jj < 4; jj++) {                                            \
      int id = 4 * lw + jj;                                                                       \
      int sg = 1 + (id >> 3), j = id & 7;                                                         \
      int k = j * 8 + r8;                                                                         \
      int c = cp ^ ((((k >> 1) & 1) | (((k >> 3) & 1) << 1)) << 1);                               \
      boff[jj] = (unsigned)(k * 6144 + (sg * 1024 + cb * 64) * 2 + c * 16);                       \
    }                                                                                             \
  }                                                                                               \
  unsigned ktv = (unsigned)(koff + (k0)) & 15;                                                    \
  const char* ap = (const char*)x + ktv * 128;                                                    \
  const char* bp = (const char*)w + (size_t)ktv * (64 * 6144);                                    \
  QADR
#define GLA(st, q) gldso<(st) * STG + (q) * 1024, XNT>(ap, aoff[q], la0);
#define GLB(st, q) gldso<WOFF + (st) * STG + (q) * 1024, WNT>(bp, boff[q], la0);
#define GLQ(sl, kc)                                                                  \
  {                                                                                  \
    unsigned kq = (unsigned)koff;                                                    \
    asm volatile("" : "+s"(kq));                                                     \
    const char* qp = (const char*)w + (size_t)((kq + (kc)) & 15) * (64 * 6144);      \
    gldso<(sl) * 8192, WNT>(qp, qoff[0], lq0); gldso<(sl) * 8192 + 1024, WNT>(qp, qoff[1], lq0); \
  }
#define QADR { RELANE const int k = lw * 16 + (lane >> 3); qoff[0] = (unsigned)(k * 6144 + cb * 128 + (((lane & 7) ^ (k & 2)) * 16)); \
               qoff[1] = (qoff[0] ^ 64u) + 8u * 6144u; asm volatile("" : "+v"(qoff[0]), "+v"(qoff[1])); }
#define NEXTK NEXTJ(1)
#define NEXTJ(n)                                           \
  asm volatile("" : "+s"(ktv));                            \
  ktv = (ktv + (n)) & 15;                                  \
  ap = (const char*)x + ktv * 128;                         \
  bp = (const char*)w + (size_t)ktv * (64 * 6144);
#define GLF(st, q) gldso<QF - WOFF + (st) * 8192 + (q) * 1024, WNT>(bp, qoff[q], lq0);
#define GLT(st) if (NF > (st)) { GLF(st, 0) GLF(st, 1) } GLA(st, 0) GLA(st, 1) GLA(st, 2) GLA(st, 3) GLB(st, 0) GLB(st, 1) GLB(st, 2) GLB(st, 3) NEXTK
#define TOUCH(vo, base) asm volatile("s_mov_b32 m0, %0\n\ts_nop 0\n\tglobal_load_lds_dword %1, %2" ::"s"(MISC + 512), "v"(vo), "s"(base) : "memory");

#define TPF(d)                                                                                                            \
  {                                                                                                                       \
    const unsigned kt = (ktv + (d)) & 15;                                                                                 \
    const int l_ = fresh_lane();                                                                                          \
    TOUCH((unsigned)((rb * 128 + (c_ >> 1) * 64 + lw * 16 + (l_ & 15)) * 2048), (const char*)x + kt * 128)                \
    TOUCH((unsigned)(((r_ >> 2) * 96 + lw * 24 + min(l_, 23)) * 2048 + cb * 128), (const char*)w + (size_t)kt * (64 * 6144)) \
  }
#define TLBW(sh)                                                                                                          \
  {                                                                                                                       \
    const char* tb = lw == 0 ? (const char*)ws : lw == 1 ? (const char*)out : lw == 2 ? (const char*)wo : (const char*)w; \
    const unsigned lim = (lw == 0 ? 16u << 20 : lw == 1 ? 8u << 20 : lw == 2 ? 2u << 20 : 6u << 20) - 128u;               \
    TOUCH(min(((unsigned)fresh_lane() << (sh)) + 4096u + (unsigned)idx * 128u, lim), tb)                                  \
    TOUCH(((unsigned)(fresh_lane() & 1) << 17) + (unsigned)(rb * 128 * 2048 + cb * 128), u)                               \
  }
#define CTOUCH                                                                                                            \
  {                                                                                                                       \
    u64 pc_;                                                                                                              \
    unsigned d_, l1_, l2_;                                                                                                \
    asm volatile("s_getpc_b64 %0\n.Lh%=:\n\ts_mov_b32 %1, .Lh%=-.Lkbeg\n\ts_mov_b32 %2, .Lkend-.Lkbeg\n\ts_mov_b32 %3, .Lkend2-.Lkbeg" \
                 : "=s"(pc_), "=s"(d_), "=s"(l1_), "=s"(l2_));                                                            \
    TOUCH(min((unsigned)(((lw * 8 + (fresh_lane() & 7)) * 32 + idx) * 128), max(l1_, l2_)), pc_ - d_)                     \
  }
#define TOUCHN(vo, base) asm volatile("s_mov_b32 m0, %0\n\ts_nop 0\n\tglobal_load_lds_dword %1, %2 nt" ::"s"(MISC + 512), "v"(vo), "s"(base) : "memory");
#define TOUCHW TOUCH
#define WTOUCH(k, n) TOUCHW((unsigned)(((idx & 3) * 256 + lw * 64 + (k) * (64 / (n)) + (fresh_lane() & (64 / (n) - 1))) * 2048 + cb2 * 128), wo)
#define GSH 0
#define TURN(k, n) int on_ = (((idx >> GSH) & ((n) - 1)) ^ (k)) - 1; asm volatile("" : "+s"(on_)); const unsigned on = (unsigned)(on_ >> 31);
#define WTS(k, n) { TURN(k, n) TOUCH((unsigned)(((idx & 3) * 256 + lw * 64 + fresh_lane()) * 2048 + cb2 * 128) & on, on ? (const char*)wo : ap + rb * (128 * 2048)) }
#define UTS(k, n) { TURN(k, n) TOUCH((unsigned)((rb * 128 + lw * 32 + (fresh_lane() & 31)) * 2048 + cb * 128) & on, on ? (const char*)u : ap + rb * (128 * 2048)) }
  u32x2 ur[8];
  char* const utp = ws + WS_UT + (size_t)(rb * UP3 + cb * UP2 + lw * UP1);
  char* const oc = o_out + wm * 4096;
#define ULD8 { RELANE const unsigned uo = (unsigned)((rb * 128 + lw * 32 + ((g - rb) & 3)) * 2048 + cb * 128 + i16 * 8); \
               _Pragma("unroll") for (int i = 0; i < 8; i++) asm volatile("global_load_dwordx2 %0, %1, %2" UNT : "=v"(ur[i]) : "v"(uo), "s"((const char*)u + i * 8192) : "memory"); }
#define UTIE(n) asm volatile("s_waitcnt vmcnt(%8)" : "+v"(ur[0]), "+v"(ur[1]), "+v"(ur[2]), "+v"(ur[3]), "+v"(ur[4]), "+v"(ur[5]), "+v"(ur[6]), "+v"(ur[7]) : "n"(n) : "memory");
#define CLRO4 { RELANE u32x4 sv_ = vv4(SENT); asm volatile("" : "+v"(sv_)); \
                st16_sc1<0>(oc, l16, sv_); st16_sc1<1024>(oc, l16, sv_); st16_sc1<2048>(oc, l16, sv_); st16_sc1<3072>(oc, l16, sv_); }
#define CLRKZ { RELANE u32x4 sv_ = vv4(SENT); asm volatile("" : "+v"(sv_)); \
                st16_sc1<0>(kv_out, l16, sv_); st16_sc1<0>(z_out, (unsigned)(wm * 64 + (lane & 3) * 16), sv_); }
#define PUBU(c)                                                                                          \
  {                                                                                                      \
    RELANE                                                                                               \
    const unsigned sel = ((c) & 1) ? 0x07060302u : 0x05040100u;                                          \
    u32x4 v;                                                                                             \
    _Pragma("unroll") for (int q = 0; q < 4; q++) v[q] = __builtin_amdgcn_perm(ur[2 * q + 1][(c) >> 1], ur[2 * q][(c) >> 1], sel); \
    st16_sc1<(c) * 1024, SNT>(utp, (unsigned)((i16 * 4 + g) * 16), v);                                                                   \
  }
  unsigned ev_a;
  u32x4 ev_s;
#define ULDA { RELANE ev_a = (unsigned)((rb * 128 + lw * 32 + ((g - rb) & 3)) * 2048 + cb * 128 + i16 * 8); asm volatile("" : "+v"(ev_a)); }
#define ULD(i) asm volatile("global_load_dwordx2 %0, %1, %2" : "=v"(ur[i]) : "v"(ev_a), "s"((const char*)u + (i) * 8192) : "memory");
#define CLRA { RELANE ev_a = l16; ev_s = vv4(SENT); asm volatile("" : "+v"(ev_a), "+v"(ev_s)); }
#define CLRO(i) st16_sc1<(i) * 1024, CNT>(oc, ev_a, ev_s);
#define CLRK(i) if (i) st16_sc1<0>(wm ? kv_out : z_out, ev_a & 112u, ev_s); else st16_sc1<0>(kv_out, ev_a, ev_s);
#define CLRH                                                                                                                     \
  {                                                                                                                              \
    RELANE                                                                                                                       \
    const int s_ = (lane >> 3) & 3;                                                                                              \
    const unsigned a_ = (s_ < 2 ? (unsigned)(WS_H1 + (cgid * 4 + wid) * 128) + s_ * (unsigned)(WS_H2 - WS_H1)                    \
                                : (unsigned)(WS_FLAG + cgid * 128) + (s_ - 2) * (unsigned)(WS_YF - WS_FLAG)) + (lane & 7) * 16u; \
    asm volatile("s_mov_b64 exec, %3\n\tglobal_store_dwordx4 %0, %1, %2 sc1\n\ts_mov_b64 exec, -1\n\ts_nop 0"                    \
                 ::"v"(a_), "v"(ev_s), "s"(ws), "s"((u64)(wid ? 0xFFFFu : HCM)) : "memory");                                     \
  }
#define HCM 0xFFFFFFFFu
#define FLAGC(region) asm volatile("s_cmp_lg_u32 %3, 0\n\ts_cbranch_scc1 1f\n\ts_mov_b64 exec, 1\n\tglobal_store_dwordx2 %0, %1, %2 sc1\n\ts_mov_b64 exec, -1\n1:" \
                                   ::"v"((unsigned)(cgid * 128)), "v"(TAGV), "s"(ws + (region)), "s"(lw) : "memory", "scc");
  const int dch = cgid * 4 + lw;
#define PREGS "v250", "v251", "v252", "v253", "v254", "v255"
  asm volatile("s_mov_b32 m0, %11\n\t"
               "global_load_ushort v254, %1, %6\n\tglobal_load_dword v250, %0, %2" PNT "\n\tglobal_load_dword v251, %0, %3" PNT "\n\t"
               "global_load_dword v252, %0, %4" PNT "\n\tglobal_load_dword v253, %0, %5" PNT "\n\t"
               "global_load_ushort v255, %1, %7\n\tglobal_load_lds_dword %9, %10"
               ::"v"((unsigned)(dch * 256 + lane * 4)), "v"((unsigned)(dch * 2)), "s"(lar), "s"(aim), "s"(cre), "s"(cim), "s"(ldt), "s"(dsk), "s"(wid),
                 "v"(min((unsigned)(((wid * 4 + (lane & 3)) * 32 + idx) * 128), (unsigned)clen)), "s"(pc0), "n"(MISC + 512)
               : "memory", "scc", PREGS);
  asm volatile("s_load_dwordx2 %0, %1, 0x0" : "=s"(ep) : "s"(ws + WS_EPOCH) : "memory");
#define PMOV(n)                                                                                                      \
  unsigned p_lar, p_aim, p_cre, p_cim, p_ldt, p_dsk;                                                                 \
  asm volatile("s_waitcnt vmcnt(%6)\n\tv_mov_b32 %0, v250\n\tv_mov_b32 %1, v251\n\tv_mov_b32 %2, v252\n\tv_mov_b32 %3, v253\n\tv_mov_b32 %4, v254\n\tv_mov_b32 %5, v255" \
               : "=v"(p_lar), "=v"(p_aim), "=v"(p_cre), "=v"(p_cim), "=v"(p_ldt), "=v"(p_dsk) : "n"(n) : "memory", PREGS);
  const int cbase = SS + lw * SCH;
  const int ctab = lw == 0 ? 2 * STG : lw == 1 ? QF + 16384 : WOFF + 2 * STG + (lw - 2) * 16 * TP;
#define TABADR(base)                                                                                       \
  unsigned wb[8];                                                                                          \
  _Pragma("unroll") for (int r = 0; r < 8; r++) wb[r] = (unsigned)((base) + r * 2 * TP + lane * 8);        \
  const unsigned rlo = (unsigned)(i16 * TP + (g & 1) * 256 + (g >> 1) * 16);     \
  unsigned radr = rlo + (base), eadr = rlo + ctab;                                                         \
  asm volatile("" : "+v"(wb[0]), "+v"(wb[1]), "+v"(wb[2]), "+v"(wb[3]), "+v"(wb[4]), "+v"(wb[5]), "+v"(wb[6]), "+v"(wb[7]), "+v"(radr), "+v"(eadr));

  if (wid >= 4) {
    RELANE
    SB();
    STAMP(15);
    TADR(2)
#define PF                                                                                                   \
    {                                                                                                        \
      const int cs = (3 - (int)((size_t)w >> 7) - (xcd >> 1) * 4) & 7;                                       \
      const int f = (c_ - (c_ > cs ? 1 : 0)) * 8 + r_;                                                       \
      const int L = f * 64 + lw * 16, j = L / 192;                                                           \
      const int kt = (4 * (j & 3) + (xcd & 3) + 2 + (j >> 2)) & 15;                                          \
      const bool on = PFS && cs < 4 && c_ != cs;                                                             \
      const unsigned pfa = on ? (unsigned)((L + (kt - j) * 192) * 2048 + ((xcd >> 1) * 4 + cs) * 128) : (unsigned)(koff * (64 * 6144) + cb * 128); \
      TOUCH(pfa + ((unsigned)(fresh_lane() & (on ? 15 : 0)) << 11), w)                                       \
    }
    asm volatile("" : "+v"(aoff[0]), "+v"(aoff[1]), "+v"(aoff[2]), "+v"(aoff[3]), "+v"(boff[0]), "+v"(boff[1]), "+v"(boff[2]), "+v"(boff[3]));
    SB();
    STAMP(6);
    __builtin_amdgcn_s_setprio(HPRIO);
    const int ubase = UB + lw * 4608 + 512;
    TABADR(cbase + 512)
    char* const yp = ws + WS_YT + (YL ? (size_t)((dch >> 6) * 16384 + (dch & 7) * 2048 + ((dch >> 3) & 7) * 256) : (size_t)dch * 4096);
    const char* const up = ws + WS_UT + (size_t)((cgid >> 4) * UP2 + lw * 1024 + (cgid & 15) * 64);
    u32x2 flg;
    SB();
     PMOV(1)
    STAMP(7);
    SB();
#define TICK(n) asm volatile("s_waitcnt vmcnt(%0)\n\ts_barrier" ::"n"(n) : "memory");
#define FLAGST(region)                                                                                                                             \
  asm volatile("s_mov_b64 exec, 1\n\ts_cmp_lg_u32 %3, 0\n\ts_cbranch_scc1 1f\n\tglobal_store_dwordx2 %0, %1, %2 sc1\n\ts_branch 2f\n"           \
               "1:\n\ts_mov_b32 m0, %4\n\ts_nop 0\n\tglobal_load_lds_dword %5, %6\n2:\n\ts_mov_b64 exec, -1"                                      \
               ::"v"((unsigned)(cgid * 128)), "v"(TAGV), "s"(ws + (region)), "s"(lw), "n"(MISC + 512), "v"(0u), "s"(ldt) : "memory", "scc");
#define FLGO (unsigned)(128 * cg_of(fresh_lane() & 15, cgid >> 4))
#define FLAGLD asm volatile("global_load_dwordx2 %0, %1, %2 sc1" : "=v"(flg) : "v"(FLGO), "s"(ws + WS_FLAG) : "memory");
#define FLAGCK(n)                                                                                  \
  {                                                                                                \
    asm volatile("s_waitcnt vmcnt(%1)" : "+v"(flg) : "n"(n) : "memory");                           \
    if (__builtin_expect(FLAGBAD, 0)) {                                                            \
      unsigned n_;                                                                                 \
      asm volatile("s_mov_b32 %1, 0x20000\n"                                                       \
                   "1:\n\ts_sleep 2\n\tglobal_load_dwordx2 %0, %3, %4 sc1\n\t"                    \
                   "s_waitcnt vmcnt(0)\n\tv_cmp_ne_u64 vcc, %0, %2\n\ts_cbranch_vccz 2f\n\ts_sub_u32 %1, %1, 1\n\ts_cbranch_scc0 1b\n2:" \
                   : "=&v"(flg), "=&s"(n_) : "v"(TAGV), "v"(FLGO), "s"(ws + WS_FLAG) : "memory", "scc", "vcc"); \
    }                                                                                              \
  }
#define SETF(i) if (fresh_lane() == 0) lflag[(i) + lw] = 1u;
#define WOALL(kh_, i_, dst)                                                                                \
  {                                                                                                        \
    RELANE2                                                                                                \
    const char* wq_ = (const char*)wo + ((kh_) * 16 + (((i_) + rot) & 15)) * 65536;                        \
    _Pragma("unroll") for (int j = 0; j < 4; j++) glds16(wq_ + j * 8192, (j & 1) ? wo_o : wo_e, (dst) + j * 1024); \
  }
#define WOSS                                                                                               \
  {                                                                                                        \
    WOALL(lw >> 1, 12 + (lw & 1), cbase + 4608)                                                            \
    RELANE2                                                                                                \
    const char* wq_ = (const char*)wo + ((lw >> 1) * 16 + ((15 + rot) & 15)) * 65536 + (lw & 1) * 16384;   \
    glds16(wq_, wo_e, SS + (lw & 2) * SCH + (lw & 1) * 2048);                                              \
    glds16(wq_ + 8192, wo_o, SS + (lw & 2) * SCH + (lw & 1) * 2048 + 1024);                                \
  }
#define WOE(list) { RELANE2 WOX list }
#define GLU4 { RELANE const unsigned ugo = (unsigned)(g * UP3 + ((lane >> 2) & 3) * UP1 + (lane & 3) * 16); \
               _Pragma("unroll") for (int j = 0; j < 4; j++) glds16sc1(up + j * (4 * UP3), ugo, ubase + j * 1024); }
#define SEL(p, m) ((m) ? hi2f(p) : lo2f(p))
#define SETA(m) arl[m] = -rbf(__builtin_amdgcn_exp2f(SEL(p_lar, m) * 1.44269504f)) * dt * 1.44269504f; air[m] = SEL(p_aim, m) * dt * 0.15915494f;
#define SETC(m) c[m] = f32x2{SEL(p_cre, m) * dt * 2.f, SEL(p_cim, m) * dt * 2.f};
#define CEXA(m, sc) mg = __builtin_amdgcn_exp2f(arl[m] * (sc)); ph = __builtin_amdgcn_fractf(air[m] * (sc));
#define CEXB(o, m, sg) o[m] = f32x2{mg * __builtin_amdgcn_cosf(ph), (sg) * mg * __builtin_amdgcn_sinf(ph)};
#define CM(o, m, a, b) o[m] = f32x2{a[m][0] * b[m][0] - a[m][1] * b[m][1], a[m][0] * b[m][1] + a[m][1] * b[m][0]};
#define PK(P, m, a) P[m] = pack2(a[m][0], a[m][1]);
#define ADVP(o, a, b, P)                                                                                   \
  {                                                                                                        \
    f32x2 o0_, o1_, t_;                                                                                    \
    asm("pkadv \"%0\", \"%1\", \"%2\", \"%3\", \"%4\", \"%5\", \"%6\", \"%7\""                                                     \
        : "=&v"(o0_), "=&v"(o1_), "=&v"(P), "=&v"(t_) : "v"(a[0]), "v"(a[1]), "v"(b[0]), "v"(b[1]));       \
    o[0] = o0_; o[1] = o1_;                                                                                \
  }
#define MF(sg, k) D[sg] = MFMA16(gf[sg][k], Ef[k], D[sg]);
#define W2(ra_, A, rb_, B)                                                         \
  asm volatile("ds_write2_b64 %0, %1, %2 offset0:%3 offset1:%4" ::"v"(wb[(ra_) >> 1]), "v"(A), "v"(B), \
               "n"(((ra_) & 1) * (TP / 8)), "n"(((rb_) - ((ra_) & ~1)) * (TP / 8)) : "memory");
#define GRD(sg, k) gf[sg][k] = *(const LAS bf16x8*)(lds + radr + (k) * 32);
#define ERD(k) Ef[k] = *(const LAS bf16x8*)(lds + eadr + (k) * 32);
#define EXP0 { RELANE *(LAS u32x2*)(lds + cbase + 2 * (2044 - i16 * 16 - 4 * g)) = k0; }
#define EXP1 *(LAS u32x2*)(lds + cbase + 2 * (2044 - 256 - i16 * 16 - 4 * g)) = u32x2{pack2(D[1][2], D[1][1]), pack2(D[1][0], D[1][0])};
#define EXP2 *(LAS u32x4*)(lds + cbase + 2 * (2040 - 512 - 256 * (g >> 1) - i16 * 16 - 8 * (g & 1))) = \
      u32x4{pack2(D[2][2], D[2][2]), pack2(D[2][1], D[2][1]), pack2(vf1(D[2][1]), D[2][0]), pack2(D[2][0], D[2][0])};
#define EXP3                                                                                       \
  {                                                                                                \
    unsigned cc = pack2(D[3][2], D[3][2]), bb = pack2(D[3][1], D[3][1]), ba = pack2(vf1(D[3][1]), D[3][0]), aa = pack2(D[3][0], D[3][0]); \
    const int o3 = cbase + 2 * (2032 - 1024 - 256 * g - i16 * 16);                                 \
    *(LAS u32x4*)(lds + o3) = u32x4{cc, cc, bb, bb};                                               \
    *(LAS u32x4*)(lds + o3 + 16) = u32x4{bb, ba, aa, aa};                                          \
  }
#define ZPAD *(LAS u32x2*)(lds + cbase + 4096 + lane * 8) = vv2(0u, 0u);
#define ZPADU *(LAS u32x2*)(lds + ubase - 512 + lane * 8) = vv2(0u, 0u);
#define LGKM0 asm volatile("s_waitcnt lgkmcnt(0)" ::: "memory");
#define KVARS                                                                              \
      f32x4 D[4];                                                                          \
      _Pragma("unroll") for (int s = 0; s < 4; s++) D[s] = f32x4{0, 0, 0, 0};              \
      float dt, mg, ph, arl[2], air[2];                                                    \
      f32x2 c[2], zH[2], e[2], h[2], z[2];                                               \
      bf16x8 Ef[8], gf[4][8];                                                              \
      u32x2 T[64];
#define BARP asm volatile("s_waitcnt lgkmcnt(8)\n\ts_barrier" ::: "memory");
#define PIN8(F) asm volatile("" : "+v"(F[0]), "+v"(F[1]), "+v"(F[2]), "+v"(F[3]), "+v"(F[4]), "+v"(F[5]), "+v"(F[6]), "+v"(F[7]));
    s16x4 kl[16], kh_[16];
#define KAL(L0)                                                                                            \
  {                                                                                                        \
    RELANE                                                                                                 \
    const unsigned ka = (unsigned)(cbase + 24 + 64 * g + 8 * ((i16 >> 2) - (i16 & 3)));                    \
    _Pragma("unroll") for (int L = (L0); L < (L0) + 4; L++) {                                              \
      kl[L] = __builtin_amdgcn_ds_read_tr16_b64_v4i16((LAS s16x4*)(size_t)(ka + 3840 - 256 * L));          \
      kh_[L] = __builtin_amdgcn_ds_read_tr16_b64_v4i16((LAS s16x4*)(size_t)(ka + 3872 - 256 * L));         \
    }                                                                                                      \
  }
    unsigned ub[2][4], yo, wox;
    int prv;
#define CPRO                                                                                               \
  {                                                                                                        \
    RELANE                                                                                                 \
    _Pragma("unroll") for (int h = 0; h < 2; h++) {                                                        \
      const int pl = i16 + 2 * g - 7 + h;                                                                  \
      _Pragma("unroll") for (int v = 0; v < 4; v++)                                                        \
        ub[h][v] = (unsigned)(ubase - 256 + (pl >> 1) * 64 + (pl & 1) * 8 + ((v + (pl >> 3)) & 3) * 16);   \
    }                                                                                                      \
    prv = (g ? lane - 16 : lane + 47) * 4;                                                                 \
    prv = lane ? prv : 252;                                                                                \
    yo = YL ? (unsigned)(((g & 1) * 2 + (i16 >> 3)) * YRP + (i16 & 7) * 32 + (g >> 1) * 16) : (unsigned)((g & 1) * 512 + i16 * 32 + (g >> 1) * 16);                                             \
    if (HWO) { RELANE2 wox = (wo_e ^ (unsigned)((wid & 1) * 64)) + (unsigned)(wid & 3) * 8192u; asm volatile("" : "+v"(wox)); } \
    asm volatile("" : "+v"(ub[0][0]), "+v"(ub[0][1]), "+v"(ub[0][2]), "+v"(ub[0][3]), "+v"(ub[1][0]), "+v"(ub[1][1]), "+v"(ub[1][2]), "+v"(ub[1][3]), "+v"(yo), "+v"(prv)); \
  }
#define KPIN PIN8(kl) PIN8((kl + 8)) PIN8(kh_) PIN8((kh_ + 8))
    {
      KVARS
      SB(); dt = rbf(__builtin_amdgcn_exp2f(lo2f(p_ldt) * 1.44269504f)); SB();
      SB(); SETA(0) SB();
      SB(); SETA(1) SB();
      SB(); SETC(0) SB();
      SB(); SETC(1) SB();
      SB(); CEXA(0, 256.f) SB();
      SB(); CEXB(zH, 0, 1.f) SB();
      SB(); CEXA(1, 256.f) SB();
      SB(); CEXB(zH, 1, 1.f) SB();
      SB(); CEXA(0, 2.f) SB();
      SB(); CEXB(z, 0, 1.f) SB();
      SB(); CEXA(1, 2.f) SB();
      SB(); CEXB(z, 1, 1.f) SB();
      SB(); ADVP(h, c, zH, T[0]) e[0] = h[0]; e[1] = h[1]; SB();
      SB(); ADVP(e, e, z, T[1]) W2(0, T[0], 1, T[1]) SB();
      SB(); ADVP(e, e, z, T[2]) W2(2, T[2], 4, T[2]) SB();
      SB(); ADVP(e, e, z, T[3]) SB();
      SB(); ADVP(e, e, z, T[4]) W2(5, T[3], 6, T[4]) SB();
      SB(); ADVP(e, e, z, T[5]) W2(8, T[4], 9, T[5]) SB();
      SB(); ADVP(e, e, z, T[6]) W2(10, T[6], 12, T[6]) SB();
      SB(); ADVP(e, e, z, T[7]) SB();
      SB(); ADVP(e, e, z, T[8]) W2(13, T[7], 14, T[8]) SB();
      SB(); CM(z, 0, z, z) CM(z, 1, z, z) GRD(1, 0) GRD(1, 1) GRD(1, 2) GRD(1, 3) SB();
      SB(); ADVP(h, h, zH, T[9]) e[0] = h[0]; e[1] = h[1]; GRD(1, 4) GRD(1, 5) GRD(1, 6) GRD(1, 7) SB();
      SB(); ADVP(e, e, z, T[10]) W2(0, T[9], 1, T[10]) SB();
      SB(); ADVP(e, e, z, T[11]) W2(2, T[11], 4, T[11]) SB();
      SB(); ADVP(e, e, z, T[12]) SB();
      SB(); ADVP(e, e, z, T[13]) W2(5, T[12], 6, T[13]) SB();
      SB(); ADVP(h, h, zH, T[14]) e[0] = h[0]; e[1] = h[1]; SB();
      SB(); ADVP(e, e, z, T[15]) W2(8, T[14], 9, T[15]) SB();
      SB(); ULD8 SB();
      SB(); ADVP(e, e, z, T[16]) W2(10, T[16], 12, T[16]) SB();
      SB(); ADVP(e, e, z, T[17]) SB();
      SB(); ADVP(e, e, z, T[18]) W2(13, T[17], 14, T[18]) SB();
      SB(); CM(z, 0, z, z) CM(z, 1, z, z) GRD(2, 0) GRD(2, 1) GRD(2, 2) GRD(2, 3) SB();
      SB(); ADVP(h, h, zH, T[19]) e[0] = h[0]; e[1] = h[1]; GRD(2, 4) GRD(2, 5) GRD(2, 6) GRD(2, 7) SB();
      SB(); ADVP(e, e, z, T[20]) W2(0, T[19], 1, T[20]) SB();
      SB(); ADVP(e, e, z, T[21]) SB();
      SB(); ADVP(h, h, zH, T[22]) e[0] = h[0]; e[1] = h[1]; W2(2, T[21], 4, T[22]) SB();
      SB(); ADVP(e, e, z, T[23]) SB();
      SB(); ADVP(e, e, z, T[24]) W2(5, T[23], 6, T[24]) SB();
      SB(); ADVP(h, h, zH, T[25]) e[0] = h[0]; e[1] = h[1]; SB();
      SB(); ADVP(e, e, z, T[26]) W2(8, T[25], 9, T[26]) SB();
      SB(); ADVP(e, e, z, T[27]) SB();
      SB(); ADVP(h, h, zH, T[28]) e[0] = h[0]; e[1] = h[1]; W2(10, T[27], 12, T[28]) SB();
      SB(); ADVP(e, e, z, T[29]) SB();
      SB(); ADVP(e, e, z, T[30]) W2(13, T[29], 14, T[30]) SB();
      SB(); GRD(3, 0) GRD(3, 1) GRD(3, 2) GRD(3, 3) GRD(3, 4) GRD(3, 5) GRD(3, 6) GRD(3, 7) SB();
      SB(); STAMP(2); BARRIER(); ERD(0) ERD(1) ERD(2) ERD(3) ERD(4) ERD(5) ERD(6) ERD(7) TAG1 TAG2 TAG3 PIN8(Ef) LGKM0 SB();
      TICK(9) STAMP(8);
      GLF(2, 0)
      GLF(2, 1)
      GLA(2, 0)
      GLA(2, 1)
      GLA(2, 2)
      GLA(2, 3)
      GLB(2, 0)
      GLB(2, 1)
      GLB(2, 2)
      GLB(2, 3) NEXTK
      PF
      SB(); MF(1, 0) MF(2, 0) MF(3, 0) SB();
      SB(); MF(1, 1) MF(2, 1) MF(3, 1) SB();
      SB(); MF(1, 2) MF(2, 2) MF(3, 2) SB();
      SB(); MF(1, 3) MF(2, 3) MF(3, 3) SB();
      SB(); MF(1, 4) MF(2, 4) MF(3, 4) SB();
      SB(); MF(1, 5) MF(2, 5) MF(3, 5) SB();
      SB(); MF(1, 6) MF(2, 6) MF(3, 6) SB();
      SB(); MF(1, 7) MF(2, 7) MF(3, 7) SB();
      TICK(20)
      GLF(0, 0)
      GLF(0, 1)
      GLA(0, 0)
      GLA(0, 1)
      GLA(0, 2)
      GLA(0, 3)
      GLB(0, 0)
      GLB(0, 1)
      GLB(0, 2)
      GLB(0, 3) NEXTK
      SB(); EXP1 SB();
      SB(); EXP2 SB();
      SB(); EXP3 SB();
      SB(); ZPAD SB();
      STAMP(5);
      TICK(11) STAMP(9);
      GLF(1, 0)
      GLF(1, 1)
      GLA(1, 0)
      GLA(1, 1)
      GLA(1, 2)
      GLA(1, 3)
      GLB(1, 0)
      GLB(1, 1)
      GLB(1, 2)
      GLB(1, 3) NEXTK
      TICK(10)
      GLF(2, 0)
      GLF(2, 1)
      GLA(2, 0)
      GLA(2, 1)
      GLA(2, 2)
      GLA(2, 3)
      GLB(2, 0)
      GLB(2, 1)
      GLB(2, 2)
      GLB(2, 3) NEXTK
      TICK(10) STAMP(10);
      GLF(0, 0)
      GLF(0, 1)
      GLA(0, 0)
      GLA(0, 1)
      GLA(0, 2)
      GLA(0, 3)
      GLB(0, 0)
      GLB(0, 1)
      GLB(0, 2)
      GLB(0, 3) NEXTK
      TICK(10)
      GLF(1, 0)
      GLF(1, 1)
      GLA(1, 0)
      GLA(1, 1)
      GLA(1, 2)
      GLA(1, 3)
      GLB(1, 0)
      GLB(1, 1)
      GLB(1, 2)
      GLB(1, 3) NEXTK
      TICK(10) STAMP(11);
      GLF(2, 0)
      GLF(2, 1)
      GLA(2, 0)
      GLA(2, 1)
      GLA(2, 2)
      GLA(2, 3)
      GLB(2, 0)
      GLB(2, 1)
      GLB(2, 2)
      GLB(2, 3) NEXTK
      TICK(10)
      GLF(0, 0)
      GLF(0, 1)
      GLA(0, 0)
      GLA(0, 1)
      GLA(0, 2)
      GLA(0, 3)
      GLB(0, 0)
      GLB(0, 1)
      GLB(0, 2)
      GLB(0, 3) NEXTK
      TICK(10) STAMP(12);
      GLA(1, 0)
      GLA(1, 1)
      GLA(1, 2)
      GLA(1, 3)
      GLB(1, 0)
      GLB(1, 1)
      GLB(1, 2)
      GLB(1, 3) NEXTK
      UTIE(63)
      SB(); PUBU(0) SB();
      SB(); PUBU(1) SB();
      TICK(10)
      GLA(2, 0)
      GLA(2, 1)
      GLA(2, 2)
      GLA(2, 3)
      GLB(2, 0)
      GLB(2, 1)
      GLB(2, 2)
      GLB(2, 3) NEXTK
      SB(); PUBU(2) SB();
      SB(); PUBU(3) SB();
      TICK(12) STAMP(13);
      GLA(0, 0)
      GLA(0, 1)
      GLA(0, 2)
      GLA(0, 3)
      GLB(0, 0)
      GLB(0, 1)
      GLB(0, 2)
      GLB(0, 3) NEXTK
      TICK(10)
      GLA(1, 0)
      GLA(1, 1)
      GLA(1, 2)
      GLA(1, 3)
      GLB(1, 0)
      GLB(1, 1)
      GLB(1, 2)
      GLB(1, 3) NEXTK
      SB(); KAL(0) SB();
      SB(); KAL(4) SB();
      TICK(8) STAMP(14); FLAGST(WS_FLAG)
      GLA(2, 0)
      GLA(2, 1)
      GLA(2, 2)
      GLA(2, 3)
      GLB(2, 0)
      GLB(2, 1)
      GLB(2, 2)
      GLB(2, 3) NEXTK
      SB(); KAL(8) SB();
      SB(); KAL(12) SB();
      TICK(9)
      GLA(0, 0)
      GLA(0, 1)
      GLA(0, 2)
      GLA(0, 3)
      GLB(0, 0)
      GLB(0, 1)
      GLB(0, 2)
      GLB(0, 3) NEXTJ(11)
      SB(); KPIN SB();
      SB(); CPRO SB();
      TICK(8)
      QADR
      GLQ(2, 13)
      GLQ(3, 14)
      GLA(1, 0)
      GLA(1, 1)
      GLA(1, 2)
      GLA(1, 3) NEXTK
      TICK(8)
      GLA(2, 0)
      GLA(2, 1)
      GLA(2, 2)
      GLA(2, 3) NEXTK
      GLQ(4, 10)
      GLQ(5, 11)
      FLAGLD
      TICK(13) STAMP(15); __builtin_amdgcn_s_setprio(1);
      GLA(0, 0)
      GLA(0, 1)
      GLA(0, 2)
      GLA(0, 3) NEXTK
      GLQ(0, 12)
      GLQ(1, 15)
      __builtin_amdgcn_s_setprio(0);
      ZPADU FLAGCK(8) GLU4
      WAIT_VM(4); SETF(44) __builtin_amdgcn_s_setprio(1);
      __builtin_amdgcn_s_setprio(0);
      WAIT_VM(0);
    }
    STAMP(1);
    {
      bf16x8 KA[16];
#pragma unroll
      for (int L = 0; L < 16; L++) KA[L] = BC8(__builtin_shufflevector(kl[L], kh_[L], 0, 1, 2, 3, 4, 5, 6, 7));
      f32x4 Y[8];
      u32x2 bf[NB][2];
      float ca[2][8];
      unsigned ry[8][2];
      const float dk = lo2f(p_dsk); (void)dk;
#define BL(b, h, v, x) bf[b][h] = *(const LAS u32x2*)(size_t)(ub[h][v] + 256 * ((x) + 1));
#define CMF(b, L, c, first) Y[c] = MFMA16(KA[L], BC8(__builtin_shufflevector(bf[b][0], bf[b][1], 0, 1, 2, 3)), ((first) ? f32x4{0, 0, 0, 0} : Y[c])); asm volatile("" : "+v"(Y[c]));
#define CIA(s, c) ca[s][c] = __builtin_bit_cast(float, __builtin_amdgcn_ds_bpermute(prv, __builtin_bit_cast(int, Y[c][0])));
#define CIB(c, j) Y[c][j] = Y[c][(j) + 1]; asm volatile("" : "+v"(Y[c]));
#define CIC(s, c) Y[c][2] = (s) < 0 ? Y[c][3] : Y[c][3] + ca[(s) & 1][c]; asm volatile("" : "+v"(Y[c]));
#define CID(c) Y[c][3] = 0.f; asm volatile("" : "+v"(Y[c]));
#define CSH(s, c) ca[s][c] = prv == 252 ? ((c) ? ca[s][((c) - 1) & 7] : 0.f) : ca[s][c]; asm volatile("" : "+v"(ca[s][c]));
#define EPA(s, c) ry[c][0] = pack2(Y[c][3] + ca[s][c], Y[c][2]); ry[c][1] = pack2(vf1(Y[c][1]), Y[c][0]); asm volatile("" : "+v"(ry[c][0]), "+v"(ry[c][1]));
#define EPC(c2)                                                                                        \
  {                                                                                                    \
    auto a = __builtin_amdgcn_permlane16_swap(ry[2 * (c2)][0], ry[2 * (c2) + 1][0], false, false);     \
    auto b = __builtin_amdgcn_permlane16_swap(ry[2 * (c2)][1], ry[2 * (c2) + 1][1], false, false);     \
    if (YPR) { SB(); __builtin_amdgcn_s_setprio(3); }                                                  \
    st16_sc1<YL ? 0 : (c2) * 1024, SNT>(yp + (YL ? (c2) * 4 * YRP : 0), yo, u32x4{a[0], b[0], a[1], b[1]}); \
    if (YPR == 1 && (c2) < 3) __builtin_amdgcn_s_setprio(CPB);                                           \
  }
      u32x4 rw[RWN];
      unsigned rwa[2];
#define RWLD(n, kh, i)                                                                        \
  {                                                                                          \
    unsigned t_;                                                                             \
    asm volatile("v_xor_b32 %1, %4, %2\n\tglobal_load_dwordx4 %0, %1, %3"                     \
                 : "=v"(rw[n]), "=&v"(t_) : "v"(woxr), "s"(wo), "n"(((kh) * 16 + (i)) << 16) : "memory"); \
  }
#define RWW(n)                                                                                \
  SB();                                                                                      \
  rwa[0] = w1k + (unsigned)fresh_lane() * 16u; rwa[1] = rwa[0] + KH;                         \
  asm volatile("" : "+v"(rwa[0]), "+v"(rwa[1]));                                             \
  lds_wait_all(MISC + (fresh_lane() & 7) * 4, 192);                                          \
  WAIT_VM(n);                                                                                \
  _Pragma("unroll") for (int j = 0; j < RWN; j++) asm volatile("" : "+v"(rw[j])::"memory");  \
  SB();
#define RWST(n, kh, i) asm volatile("ds_write_b128 %0, %1 offset:%2" ::"v"(rwa[kh]), "v"(rw[n]), "n"((i) * 4096) : "memory"); SB();
      SB();
#define CS(i)
      __builtin_amdgcn_s_setprio(1);
      SB(); BL(0, 0, 2, -1) BL(0, 1, 2, -1) BL(1, 0, 1, 14) BL(1, 1, 1, 14) SB(); CMF(0, 1, 0, 1) BL(2, 0, 3, 0) BL(2, 1, 3, 0) SB();
      SB(); CMF(0, 3, 1, 1) BL(3, 0, 0, 13) BL(3, 1, 0, 13) SB();
      SB(); CMF(0, 5, 2, 1) BL(4, 0, 0, 1) BL(4, 1, 0, 1) SB();
      SB(); CMF(0, 7, 3, 1) BL(5, 0, 3, 12) BL(5, 1, 3, 12) SB();
      SB(); CMF(0, 9, 4, 1) SB();
      SB(); CMF(0, 11, 5, 1) SB();
      SB(); CMF(0, 13, 6, 1) SB();
      SB(); CMF(0, 15, 7, 1) BL(0, 0, 1, 2) BL(0, 1, 1, 2) SB();
      SB(); CMF(1, 0, 7, 0) BL(1, 0, 2, 11) BL(1, 1, 2, 11) SB();
      SB(); CMF(2, 0, 0, 0) SB();
      SB(); CMF(2, 2, 1, 0) SB();
      SB(); CMF(2, 4, 2, 0) SB();
      SB(); CMF(2, 6, 3, 0) SB();
      SB(); CMF(2, 8, 4, 0) SB();
      SB(); CMF(2, 10, 5, 0) SB();
      SB(); CMF(2, 12, 6, 0) SB();
      SB(); CMF(2, 14, 7, 0) BL(2, 0, 2, 3) BL(2, 1, 2, 3) SB();
      SB(); CMF(3, 1, 7, 0) BL(3, 0, 1, 10) BL(3, 1, 1, 10) SB();
      SB(); CMF(4, 1, 1, 0) SB();
      SB(); CMF(4, 3, 2, 0) SB();
      SB(); CMF(4, 5, 3, 0) SB();
      SB(); CMF(4, 7, 4, 0) SB();
      SB(); CMF(4, 9, 5, 0) SB();
      SB(); CMF(4, 11, 6, 0) SB();
      WOSS

      SB(); CMF(4, 13, 7, 0) BL(4, 0, 3, 4) BL(4, 1, 3, 4) SB();
      WTOUCH(0, 1)
      SB(); CMF(5, 0, 6, 0) SB();
      SB(); CMF(5, 2, 7, 0) BL(5, 0, 0, 9) BL(5, 1, 0, 9) SB();
      SB(); CMF(0, 0, 1, 0) SB();
      SB(); CMF(0, 2, 2, 0) SB();
      SB(); CMF(0, 4, 3, 0) CIA(1, 0) SB();
      WOXR
      SB(); RWLD(0, 0, 0) SB();
      SB(); CMF(0, 6, 4, 0) CIB(0, 0) CIB(0, 1) SB();
      SB(); CMF(0, 8, 5, 0) CIC(-1, 0) CID(0) SB();
      SB(); CMF(0, 10, 6, 0) SB();
      SB(); CMF(0, 12, 7, 0) BL(0, 0, 0, 5) BL(0, 1, 0, 5) SB();
      SB(); RWLD(1, 1, 0) SB();
      SB(); CMF(1, 1, 6, 0) CIA(1, 1) CIB(1, 0) SB();
      SB(); CMF(1, 3, 7, 0) BL(1, 0, 3, 8) BL(1, 1, 3, 8) SB();
      __builtin_amdgcn_s_setprio(0);
      SB(); CMF(2, 1, 2, 0) CIB(1, 1) CIC(-1, 1) SB();
      SB(); CMF(2, 3, 3, 0) CID(1) SB();
      SB(); RWLD(2, 0, 1) SB();
      SB(); CMF(2, 5, 4, 0) SB();
      SB(); CMF(2, 7, 5, 0) SB();
      SB(); CMF(2, 9, 6, 0) SB();
      SB(); CMF(2, 11, 7, 0) BL(2, 0, 1, 6) BL(2, 1, 1, 6) SB();
      SB(); RWLD(3, 1, 1) SB();
      SB(); CMF(3, 0, 5, 0) SB();
      SB(); CMF(3, 2, 6, 0) SB();
      SB(); CMF(3, 4, 7, 0) BL(3, 0, 2, 7) BL(3, 1, 2, 7) SB();
      SB(); CMF(4, 0, 2, 0) SB();
      SB(); RWLD(4, 0, 2) SB();
      SB(); CMF(4, 2, 3, 0) SB();
      SB(); CMF(4, 4, 4, 0) SB();
      SB(); CMF(4, 6, 5, 0) SB();
      SB(); CMF(4, 8, 6, 0) SB();
      SB(); RWLD(5, 1, 2) SB();
      SB(); CMF(4, 10, 7, 0) BL(4, 0, 1, -1) BL(4, 1, 1, -1) SB();
      SB(); CMF(5, 1, 5, 0) CIA(1, 2) CIB(2, 0) SB();
      SB(); CMF(5, 3, 6, 0) CIB(2, 1) CIC(-1, 2) SB();
      SB(); CMF(5, 5, 7, 0) BL(5, 0, 0, 14) BL(5, 1, 0, 14) SB();
      SB(); RWLD(6, 0, 3) SB();
      SB(); CMF(0, 1, 3, 0) CID(2) SB();
      SB(); CMF(0, 3, 4, 0) SB();
      SB(); CMF(0, 5, 5, 0) SB();
      SB(); CMF(0, 7, 6, 0) SB();
      SB(); RWLD(7, 1, 3) SB();
      SB(); CMF(0, 9, 7, 0) BL(0, 0, 2, 0) BL(0, 1, 2, 0) SB();
      SB(); CMF(1, 0, 4, 0) SB();
      SB(); CMF(1, 2, 5, 0) SB();
      SB(); CMF(1, 4, 6, 0) SB();
      SB(); RWLD(8, 0, 4) SB();
      SB(); CMF(1, 6, 7, 0) BL(1, 0, 3, 13) BL(1, 1, 3, 13) SB();
      SB(); CMF(2, 0, 3, 0) SB();
      SB(); CMF(2, 2, 4, 0) SB();
      SB(); CMF(2, 4, 5, 0) SB();
      SB(); RWLD(9, 1, 4) SB();
      SB(); CMF(2, 6, 6, 0) SB();
      SB(); CMF(2, 8, 7, 0) BL(2, 0, 3, 1) BL(2, 1, 3, 1) SB();
      SB(); CMF(3, 1, 4, 0) SB();
      SB(); CMF(3, 3, 5, 0) CIA(1, 3) CIB(3, 0) SB();
      SB(); CMF(3, 5, 6, 0) CIB(3, 1) CIC(-1, 3) SB();
      SB(); CMF(3, 7, 7, 0) CID(3) CIA(1, 4) SB();
      SB(); CMF(4, 1, 0, 0) CIB(4, 0) CIB(4, 1) SB();
      SB(); CMF(4, 3, 1, 0) CIC(-1, 4) CID(4) SB();
      SB(); CMF(4, 5, 2, 0) CIA(1, 5) CIB(5, 0) SB();
      SB(); CMF(4, 7, 3, 0) CIB(5, 1) CIC(-1, 5) CID(5) CIA(1, 6) SB();
      SB(); CMF(4, 9, 4, 0) CIB(6, 0) CIB(6, 1) CIC(-1, 6) CID(6) CIA(1, 7) SB();
      SB(); CMF(4, 11, 5, 0) CIB(7, 0) CIB(7, 1) CIC(-1, 7) CID(7) SB();
      SB(); CMF(4, 13, 6, 0) BL(3, 0, 2, 12) BL(3, 1, 2, 12) SB();
      SB(); CMF(4, 15, 7, 0) BL(4, 0, 0, 2) BL(4, 1, 0, 2) SB();
      SB(); CMF(5, 0, 7, 0) BL(5, 0, 1, 11) BL(5, 1, 1, 11) SB();
      SB(); CMF(0, 0, 0, 0) SB();
      SB(); CMF(0, 2, 1, 0) SB();
      SB(); CMF(0, 4, 2, 0) SB();
      SB(); CMF(0, 6, 3, 0) CSH(1, 7) CSH(1, 6) SB();
      SB(); CMF(0, 8, 4, 0) CSH(1, 5) CSH(1, 4) SB();
      SB(); CMF(0, 10, 5, 0) CSH(1, 3) CSH(1, 2) SB();
      SB(); CMF(0, 12, 6, 0) CSH(1, 1) CSH(1, 0) SB();
      SB(); CMF(0, 14, 7, 0) BL(0, 0, 1, 3) BL(0, 1, 1, 3) SB();
      SB(); CMF(1, 1, 7, 0) BL(1, 0, 0, 10) BL(1, 1, 0, 10) SB();
      SB(); CMF(2, 1, 1, 0) SB();
      SB(); CMF(2, 3, 2, 0) SB();
      SB(); CMF(2, 5, 3, 0) SB();
      SB(); CMF(2, 7, 4, 0) SB();
      SB(); CMF(2, 9, 5, 0) SB();
      SB(); CMF(2, 11, 6, 0) SB();
      SB(); CMF(2, 13, 7, 0) BL(2, 0, 2, 4) BL(2, 1, 2, 4) SB();
      SB(); CMF(3, 0, 6, 0) SB();
      SB(); CMF(3, 2, 7, 0) BL(3, 0, 3, 9) BL(3, 1, 3, 9) SB();
      SB(); CMF(4, 0, 1, 0) SB();
      CTOUCH
      SB(); CMF(4, 2, 2, 0) SB();
      SB(); CMF(4, 4, 3, 0) CIA(0, 0) SB();
      SB(); CMF(4, 6, 4, 0) CIB(0, 0) CIB(0, 1) SB();
      SB(); CMF(4, 8, 5, 0) CIC(1, 0) CID(0) SB();
      SB(); CMF(4, 10, 6, 0) SB();
      SB(); CMF(4, 12, 7, 0) BL(4, 0, 3, 5) BL(4, 1, 3, 5) SB();
      SB(); CMF(5, 1, 6, 0) CIA(0, 1) CIB(1, 0) SB();
      SB(); CMF(5, 3, 7, 0) BL(5, 0, 2, 8) BL(5, 1, 2, 8) SB();
      SB(); CMF(0, 1, 2, 0) CIB(1, 1) CIC(1, 1) SB();
      SB(); CMF(0, 3, 3, 0) CID(1) SB();
      RWW(1)
      RWST(0, 0, 0)
      RWST(1, 1, 0)
      SB(); CMF(0, 5, 4, 0) SB();
      RWST(2, 0, 1)
      RWST(3, 1, 1)
      SB(); CMF(0, 7, 5, 0) SB();
      RWST(4, 0, 2)
      RWST(5, 1, 2)
      SB(); CMF(0, 9, 6, 0) SB();
      RWST(6, 0, 3)
      RWST(7, 1, 3)
      SB(); CMF(0, 11, 7, 0) BL(0, 0, 0, 6) BL(0, 1, 0, 6) SB();
      RWST(8, 0, 4)
      RWST(9, 1, 4) SETF(32) STAMP(5);
      SB(); CMF(1, 0, 5, 0) SB();
      SB(); CMF(1, 2, 6, 0) SB();
      SB(); CMF(1, 4, 7, 0) BL(1, 0, 1, 7) BL(1, 1, 1, 7) SB();
      SB(); CMF(2, 0, 2, 0) SB();
      SB(); CMF(2, 2, 3, 0) SB();
      SB(); CMF(2, 4, 4, 0) SB();
      SB(); CMF(2, 6, 5, 0) SB();
      SB(); CMF(2, 8, 6, 0) SB();
      SB(); CMF(2, 10, 7, 0) BL(2, 0, 0, -1) BL(2, 1, 0, -1) SB();
      SB(); CMF(3, 1, 5, 0) CIA(0, 2) CIB(2, 0) SB();
      SB(); CMF(3, 3, 6, 0) CIB(2, 1) CIC(1, 2) SB();
      SB(); CMF(3, 5, 7, 0) BL(3, 0, 3, 14) BL(3, 1, 3, 14) SB();
      SB(); CMF(4, 1, 3, 0) CID(2) SB();
      SB(); CMF(4, 3, 4, 0) SB();
      SB(); CMF(4, 5, 5, 0) SB();
      SB(); CMF(4, 7, 6, 0) SB();
      SB(); CMF(4, 9, 7, 0) BL(4, 0, 1, 0) BL(4, 1, 1, 0) SB();
      SB(); CMF(5, 0, 4, 0) SB();
      SB(); CMF(5, 2, 5, 0) SB();
      SB(); CMF(5, 4, 6, 0) SB();
      SB(); CMF(5, 6, 7, 0) BL(5, 0, 2, 13) BL(5, 1, 2, 13) SB();
      SB(); CMF(0, 0, 3, 0) SB();
      SB(); CMF(0, 2, 4, 0) SB();
      SB(); CMF(0, 4, 5, 0) SB();
      SB(); CMF(0, 6, 6, 0) SB();
      SB(); CMF(0, 8, 7, 0) BL(0, 0, 2, 1) BL(0, 1, 2, 1) SB();
      SB(); CMF(1, 1, 4, 0) SB();
      SB(); CMF(1, 3, 5, 0) CIA(0, 3) CIB(3, 0) SB();
      SB(); CMF(1, 5, 6, 0) CIB(3, 1) CIC(1, 3) SB();
      SB(); CMF(1, 7, 7, 0) CID(3) CIA(0, 4) SB();
      SB(); CMF(2, 1, 0, 0) CIB(4, 0) CIB(4, 1) SB();
      SB(); CMF(2, 3, 1, 0) CIC(1, 4) CID(4) SB();
      SB(); CMF(2, 5, 2, 0) CIA(0, 5) CIB(5, 0) SB();
      SB(); CMF(2, 7, 3, 0) CIB(5, 1) CIC(1, 5) CID(5) CIA(0, 6) SB();
      SB(); CMF(2, 9, 4, 0) CIB(6, 0) CIB(6, 1) CIC(1, 6) CID(6) CIA(0, 7) SB();
      SB(); CMF(2, 11, 5, 0) CIB(7, 0) CIB(7, 1) CIC(1, 7) CID(7) SB();
      __builtin_amdgcn_s_setprio(1);
      SB(); CMF(2, 13, 6, 0) BL(1, 0, 1, 12) BL(1, 1, 1, 12) SB();
      SB(); CMF(2, 15, 7, 0) BL(2, 0, 3, 2) BL(2, 1, 3, 2) SB();
      SB(); CMF(3, 0, 7, 0) BL(3, 0, 0, 11) BL(3, 1, 0, 11) SB();
      SB(); CMF(4, 0, 0, 0) SB();
      SB(); CMF(4, 2, 1, 0) SB();
      SB(); CMF(4, 4, 2, 0) SB();
      SB(); CMF(4, 6, 3, 0) CSH(0, 7) CSH(0, 6) SB();
      SB(); CMF(4, 8, 4, 0) CSH(0, 5) CSH(0, 4) SB();
      SB(); CMF(4, 10, 5, 0) CSH(0, 3) CSH(0, 2) SB();
      SB(); CMF(4, 12, 6, 0) CSH(0, 1) CSH(0, 0) SB();
      SB(); CMF(4, 14, 7, 0) BL(4, 0, 0, 3) BL(4, 1, 0, 3) SB();
      SB(); CMF(5, 1, 7, 0) BL(5, 0, 3, 10) BL(5, 1, 3, 10) SB();
      SB(); CMF(0, 1, 1, 0) SB();
      SB(); CMF(0, 3, 2, 0) SB();
      SB(); CMF(0, 5, 3, 0) SB();
      SB(); CMF(0, 7, 4, 0) SB();
      SB(); CMF(0, 9, 5, 0) SB();
      SB(); CMF(0, 11, 6, 0) SB();
      SB(); CMF(0, 13, 7, 0) BL(0, 0, 1, 4) BL(0, 1, 1, 4) SB();
      SB(); CMF(1, 0, 6, 0) SB();
      SB(); CMF(1, 2, 7, 0) BL(1, 0, 2, 9) BL(1, 1, 2, 9) SB();
      SB(); CMF(2, 0, 1, 0) SB();
      SB(); CMF(2, 2, 2, 0) SB();
      SB(); CMF(2, 4, 3, 0) CIA(1, 0) SB();
      SB(); CMF(2, 6, 4, 0) CIB(0, 0) CIB(0, 1) SB();
      SB(); CMF(2, 8, 5, 0) CIC(0, 0) CID(0) SB();
      SB(); CMF(2, 10, 6, 0) SB();
      SB(); CMF(2, 12, 7, 0) BL(2, 0, 2, 5) BL(2, 1, 2, 5) SB();
      SB(); CMF(3, 1, 6, 0) CIA(1, 1) CIB(1, 0) SB();
      SB(); CMF(3, 3, 7, 0) BL(3, 0, 1, 8) BL(3, 1, 1, 8) SB();
      SB(); CMF(4, 1, 2, 0) CIB(1, 1) CIC(0, 1) SB();
      SB(); CMF(4, 3, 3, 0) CID(1) SB();
      SB(); CMF(4, 5, 4, 0) SB();
      SB(); CMF(4, 7, 5, 0) SB();
      SB(); CMF(4, 9, 6, 0) SB();
      SB(); CMF(4, 11, 7, 0) BL(4, 0, 3, 6) BL(4, 1, 3, 6) SB();
      SB(); CMF(5, 0, 5, 0) SB();
      SB(); CMF(5, 2, 6, 0) SB();
      SB(); CMF(5, 4, 7, 0) BL(5, 0, 0, 7) BL(5, 1, 0, 7) SB();
      SB(); CMF(0, 0, 2, 0) SB();
      SB(); CMF(0, 2, 3, 0) SB();
      SB(); CMF(0, 4, 4, 0) SB();
      SB(); CMF(0, 6, 5, 0) SB();
      SB(); CMF(0, 8, 6, 0) SB();
      SB(); CMF(0, 10, 7, 0) BL(0, 0, 3, -1) BL(0, 1, 3, -1) SB();
      SB(); CMF(1, 1, 5, 0) CIA(1, 2) CIB(2, 0) SB();
      SB(); CMF(1, 3, 6, 0) CIB(2, 1) CIC(0, 2) SB();
      SB(); CMF(1, 5, 7, 0) BL(1, 0, 0, 0) BL(1, 1, 0, 0) SB();
      SB(); CMF(2, 1, 3, 0) CID(2) SB();
      SB(); CMF(2, 3, 4, 0) SB();
      SB(); CMF(2, 5, 5, 0) SB();
      SB(); CMF(2, 7, 6, 0) SB();
      SB(); CMF(2, 9, 7, 0) BL(2, 0, 1, 1) BL(2, 1, 1, 1) SB();
      SB(); CMF(3, 0, 4, 0) SB();
      SB(); CMF(3, 2, 5, 0) SB();
      SB(); CMF(3, 4, 6, 0) SB();
      SB(); CMF(3, 6, 7, 0) BL(3, 0, 2, 2) BL(3, 1, 2, 2) SB();
      SB(); CMF(4, 0, 3, 0) SB();
      SB(); CMF(4, 2, 4, 0) SB();
      SB(); CMF(4, 4, 5, 0) SB();
      SB(); CMF(4, 6, 6, 0) SB();
      SB(); CMF(4, 8, 7, 0) BL(4, 0, 3, 3) BL(4, 1, 3, 3) SB();
      SB(); CMF(5, 1, 4, 0) SB();
      SB(); CMF(5, 3, 5, 0) CIA(1, 3) CIB(3, 0) SB();
      SB(); CMF(5, 5, 6, 0) CIB(3, 1) CIC(0, 3) SB();
      SB(); CMF(5, 7, 7, 0) CID(3) CIA(1, 4) SB();
      SB(); CMF(0, 1, 0, 0) CIB(4, 0) CIB(4, 1) SB();
      SB(); CMF(0, 3, 1, 0) CIC(0, 4) CID(4) SB();
      SB(); CMF(0, 5, 2, 0) CIA(1, 5) CIB(5, 0) SB();
      SB(); CMF(0, 7, 3, 0) CIB(5, 1) CIC(0, 5) CID(5) CIA(1, 6) SB();
      SB(); CMF(0, 9, 4, 0) CIB(6, 0) CIB(6, 1) CIC(0, 6) CID(6) CIA(1, 7) SB();
      SB(); CMF(0, 11, 5, 0) CIB(7, 0) CIB(7, 1) CIC(0, 7) CID(7) SB();
      SB(); CMF(0, 13, 6, 0) BL(5, 0, 0, 4) BL(5, 1, 0, 4) SB();
      SB(); CMF(0, 15, 7, 0) BL(0, 0, 1, 5) BL(0, 1, 1, 5) SB();
      SB(); CMF(1, 0, 0, 0) SB();
      SB(); CMF(1, 2, 1, 0) SB();
      SB(); CMF(1, 4, 2, 0) SB();
      SB(); CMF(1, 6, 3, 0) SB();
      SB(); CMF(1, 8, 4, 0) CSH(1, 7) CSH(1, 6) SB();
      SB(); CMF(1, 10, 5, 0) CSH(1, 5) CSH(1, 4) SB();
      SB(); CMF(1, 12, 6, 0) CSH(1, 3) CSH(1, 2) SB();
      SB(); CMF(1, 14, 7, 0) CSH(1, 1) CSH(1, 0) EPA(1, 0) SB();
      SB(); CMF(2, 1, 1, 0) BL(1, 0, 2, 6) BL(1, 1, 2, 6) SB();
      SB(); CMF(2, 3, 2, 0) SB();
      SB(); CMF(2, 5, 3, 0) SB();
      SB(); CMF(2, 7, 4, 0) SB();
      SB(); CMF(2, 9, 5, 0) SB();
      SB(); CMF(2, 11, 6, 0) SB();
      SB(); CMF(2, 13, 7, 0) BL(2, 0, 3, 7) BL(2, 1, 3, 7) SB();
      SB(); CMF(3, 0, 1, 0) SB();
      SB(); CMF(3, 2, 2, 0) SB();
      SB(); CMF(3, 4, 3, 0) SB();
      SB(); CMF(3, 6, 4, 0) EPA(1, 1) SB();
      SB(); CMF(3, 8, 5, 0) EPC(0) SB();
      SB(); CMF(3, 10, 6, 0) SB();
      SB(); CMF(3, 12, 7, 0) BL(3, 0, 0, 8) BL(3, 1, 0, 8) SB();
      SB(); CMF(4, 1, 2, 0) SB();
      SB(); CMF(4, 3, 3, 0) SB();
      SB(); CMF(4, 5, 4, 0) SB();
      SB(); CMF(4, 7, 5, 0) SB();
      SB(); CMF(4, 9, 6, 0) SB();
      SB(); CMF(4, 11, 7, 0) BL(4, 0, 1, 9) BL(4, 1, 1, 9) SB();
      SB(); CMF(5, 0, 2, 0) SB();
      SB(); CMF(5, 2, 3, 0) SB();
      SB(); CMF(5, 4, 4, 0) SB();
      SB(); CMF(5, 6, 5, 0) EPA(1, 2) SB();
      SB(); CMF(5, 8, 6, 0) SB();
      SB(); CMF(5, 10, 7, 0) BL(5, 0, 2, 10) BL(5, 1, 2, 10) SB();
      SB(); CMF(0, 1, 3, 0) SB();
      SB(); CMF(0, 3, 4, 0) SB();
      SB(); CMF(0, 5, 5, 0) SB();
      SB(); CMF(0, 7, 6, 0) SB();
      SB(); CMF(0, 9, 7, 0) BL(0, 0, 3, 11) BL(0, 1, 3, 11) SB();
      SB(); CMF(1, 0, 3, 0) SB();
      SB(); CMF(1, 2, 4, 0) SB();
      SB(); CMF(1, 4, 5, 0) SB();
      SB(); CMF(1, 6, 6, 0) EPA(1, 3) SB();
      SB(); CMF(1, 8, 7, 0) EPC(1) SB();
      SB(); CMF(2, 1, 4, 0) BL(1, 0, 0, 12) BL(1, 1, 0, 12) SB();
      SB(); CMF(2, 3, 5, 0) SB();
      SB(); CMF(2, 5, 6, 0) SB();
      SB(); CMF(2, 7, 7, 0) BL(2, 0, 1, 13) BL(2, 1, 1, 13) SB();
      SB(); CMF(3, 0, 4, 0) SB();
      SB(); CMF(3, 2, 5, 0) SB();
      SB(); CMF(3, 4, 6, 0) SB();
      SB(); CMF(3, 6, 7, 0) BL(3, 0, 2, 14) BL(3, 1, 2, 14) SB();
      SB(); CMF(4, 1, 5, 0) EPA(1, 4) SB();
      SB(); CMF(4, 3, 6, 0) SB();
      SB(); CMF(4, 5, 7, 0) SB();
      SB(); CMF(5, 0, 5, 0) SB();
      SB(); CMF(5, 2, 6, 0) SB();
      SB(); CMF(5, 4, 7, 0) SB();
      SB(); CMF(0, 1, 6, 0) EPA(1, 5) SB();
      SB(); CMF(0, 3, 7, 0) EPC(2) SB();
      SB(); CMF(1, 0, 6, 0) SB();
      SB(); CMF(1, 2, 7, 0) SB();
      SB(); CMF(2, 1, 7, 0) SB();
      SB(); CMF(3, 0, 7, 0) EPA(1, 6) SB();  SB();  SB();  EPA(1, 7) SB();  EPC(3) SB();
      STAMP(3);
      {
      RELANE2
      WAIT_VM(0);
      CS(6)
      if (lane == 0) {
        if (__hip_atomic_fetch_add((LAS unsigned*)(lds + MISC) + 42, 1u, __ATOMIC_RELAXED, __HIP_MEMORY_SCOPE_WORKGROUP) == 3u)
          asm volatile("global_store_dwordx2 %0, %1, %2 sc1" ::"v"((unsigned)(cgid * 128)), "v"(TAGV), "s"(ws + WS_YF) : "memory");
      }
      __builtin_amdgcn_s_setprio(YPL);
      if (YL == 2 && lw >= 2) return;
      if (!YP) lds_wait4(lflag + 16);
      if (YP == 2) lds_wait4(lflag + 8);
      {
        const unsigned go = (unsigned)((cb * 64 + cp * 8) * 4096 + (rb * 128 + lw * 32 + r8 * 4) * 2);
        const char* yb = ws + WS_YT + 4 * 4096;
        u32x2 yv[8];
        int spins = 0;
        while (true) {
          u32x2 f;
          asm volatile("global_load_dwordx2 %0, %1, %2 sc1\n\ts_waitcnt vmcnt(0)" : "=v"(f) : "v"((unsigned)((cb * 16 + i16) * 128)), "s"(ws + WS_YF) : "memory");
          if (__builtin_amdgcn_ballot_w64(f[0] != tag[0] || f[1] != tag[1]) == 0ull) break;
          if (++spins > SPIN_CAP) break;
          __builtin_amdgcn_s_sleep(4);
        }
#define YSP 0
        if (YSP) {
          u64 dm;
          const char* yq = ws + WS_YT + (size_t)(rb * YRP + cb * 16384 + lw * 4096);
          if (YSP == 1)
          asm volatile("s_load_dwordx2 %0, %1, 0x7c\n\t""s_load_dwordx2 %0, %1, 0x17c\n\t""s_load_dwordx2 %0, %1, 0x27c\n\t""s_load_dwordx2 %0, %1, 0x37c\n\t"
                       "s_load_dwordx2 %0, %1, 0x47c\n\t""s_load_dwordx2 %0, %1, 0x57c\n\t""s_load_dwordx2 %0, %1, 0x67c\n\t""s_load_dwordx2 %0, %1, 0x77c\n\t"
                       "s_load_dwordx2 %0, %1, 0x87c\n\t""s_load_dwordx2 %0, %1, 0x97c\n\t""s_load_dwordx2 %0, %1, 0xa7c\n\t""s_load_dwordx2 %0, %1, 0xb7c\n\t"
                       "s_load_dwordx2 %0, %1, 0xc7c\n\t""s_load_dwordx2 %0, %1, 0xd7c\n\t""s_load_dwordx2 %0, %1, 0xe7c\n\t""s_load_dwordx2 %0, %1, 0xf7c\n\t"
                       "s_waitcnt lgkmcnt(0)" : "=&s"(dm) : "s"(yq) : "memory");
          else
          asm volatile("s_load_dwordx2 %0, %1, 0x3c\n\t""s_load_dwordx2 %0, %1, 0xbc\n\t""s_load_dwordx2 %0, %1, 0x13c\n\t""s_load_dwordx2 %0, %1, 0x1bc\n\t"
                       "s_load_dwordx2 %0, %1, 0x23c\n\t""s_load_dwordx2 %0, %1, 0x2bc\n\t""s_load_dwordx2 %0, %1, 0x33c\n\t""s_load_dwordx2 %0, %1, 0x3bc\n\t"
                       "s_load_dwordx2 %0, %1, 0x43c\n\t""s_load_dwordx2 %0, %1, 0x4bc\n\t""s_load_dwordx2 %0, %1, 0x53c\n\t""s_load_dwordx2 %0, %1, 0x5bc\n\t"
                       "s_load_dwordx2 %0, %1, 0x63c\n\t""s_load_dwordx2 %0, %1, 0x6bc\n\t""s_load_dwordx2 %0, %1, 0x73c\n\t""s_load_dwordx2 %0, %1, 0x7bc\n\t"
                       "s_load_dwordx2 %0, %1, 0x83c\n\t""s_load_dwordx2 %0, %1, 0x8bc\n\t""s_load_dwordx2 %0, %1, 0x93c\n\t""s_load_dwordx2 %0, %1, 0x9bc\n\t"
                       "s_load_dwordx2 %0, %1, 0xa3c\n\t""s_load_dwordx2 %0, %1, 0xabc\n\t""s_load_dwordx2 %0, %1, 0xb3c\n\t""s_load_dwordx2 %0, %1, 0xbbc\n\t"
                       "s_load_dwordx2 %0, %1, 0xc3c\n\t""s_load_dwordx2 %0, %1, 0xcbc\n\t""s_load_dwordx2 %0, %1, 0xd3c\n\t""s_load_dwordx2 %0, %1, 0xdbc\n\t"
                       "s_load_dwordx2 %0, %1, 0xe3c\n\t""s_load_dwordx2 %0, %1, 0xebc\n\t""s_load_dwordx2 %0, %1, 0xf3c\n\t""s_load_dwordx2 %0, %1, 0xfbc\n\t"
                       "s_waitcnt lgkmcnt(0)" : "=&s"(dm) : "s"(yq) : "memory");
          CS(13)
        }
        if (YP) lds_wait4(lflag + 16);
        CS(15)
#define YGP 3
        __builtin_amdgcn_s_setprio(YGP);
        if (YL == 2) {
          u32x4 yw[8];
          asm volatile(
            "global_load_dwordx4 %0, %8, %9 offset:-4096 sc1\n\tglobal_load_dwordx4 %1, %8, %9 offset:-2048 sc1\n\t"
            "global_load_dwordx4 %2, %8, %9 sc1\n\tglobal_load_dwordx4 %3, %8, %9 offset:2048 sc1\n\t"
            "global_load_dwordx4 %4, %8, %10 offset:-4096 sc1\n\tglobal_load_dwordx4 %5, %8, %10 offset:-2048 sc1\n\t"
            "global_load_dwordx4 %6, %8, %10 sc1\n\tglobal_load_dwordx4 %7, %8, %10 offset:2048 sc1\n\ts_waitcnt vmcnt(0)"
            : "=&v"(yw[0]), "=&v"(yw[1]), "=&v"(yw[2]), "=&v"(yw[3]), "=&v"(yw[4]), "=&v"(yw[5]), "=&v"(yw[6]), "=&v"(yw[7])
            : "v"((unsigned)(rb * YRP + cb * 16384 + cp * 256 + lw * 128 + r8 * 16)), "s"(ws + WS_YT + 4096), "s"(ws + WS_YT + 12288)
            : "memory");
          CS(13)
          const unsigned oo = (unsigned)((rb * 128 + lw * 64 + r8 * 8) * 4096 + 2048 + (cb * 64 + cp * 8) * 2);
#pragma unroll
          for (int i = 0; i < 8; i++) {
            const unsigned sel = (i & 1) ? 0x07060302u : 0x05040100u;
            u32x4 v;
#pragma unroll
            for (int q = 0; q < 4; q++) v[q] = __builtin_amdgcn_perm(yw[2 * q + 1][i >> 1], yw[2 * q][i >> 1], sel);
            st16_sc1<0, ONT>((char*)out + i * 4096, oo, v);
          }
        } else {
        if (YL)
        asm volatile(
            "global_load_dwordx2 %0, %8, %9 offset:-4096 sc1\n\tglobal_load_dwordx2 %1, %8, %9 offset:-2048 sc1\n\t"
            "global_load_dwordx2 %2, %8, %9 sc1\n\tglobal_load_dwordx2 %3, %8, %9 offset:2048 sc1\n\t"
            "global_load_dwordx2 %4, %8, %10 offset:-4096 sc1\n\tglobal_load_dwordx2 %5, %8, %10 offset:-2048 sc1\n\t"
            "global_load_dwordx2 %6, %8, %10 sc1\n\tglobal_load_dwordx2 %7, %8, %10 offset:2048 sc1\n\ts_waitcnt vmcnt(0)"
            : "=&v"(yv[0]), "=&v"(yv[1]), "=&v"(yv[2]), "=&v"(yv[3]), "=&v"(yv[4]), "=&v"(yv[5]), "=&v"(yv[6]), "=&v"(yv[7])
            : "v"((unsigned)(rb * YRP + cb * 16384 + cp * 256 + (lw * 32 + r8 * 4) * 2)), "s"(ws + WS_YT + 4096), "s"(ws + WS_YT + 12288)
            : "memory");
        else
        asm volatile(
            "global_load_dwordx2 %0, %8, %9 sc1\n\tglobal_load_dwordx2 %1, %10, %9 sc1\n\t"
            "global_load_dwordx2 %2, %11, %9 sc1\n\tglobal_load_dwordx2 %3, %12, %9 sc1\n\t"
            "global_load_dwordx2 %4, %8, %13 sc1\n\tglobal_load_dwordx2 %5, %10, %13 sc1\n\t"
            "global_load_dwordx2 %6, %11, %13 sc1\n\tglobal_load_dwordx2 %7, %12, %13 sc1\n\ts_waitcnt vmcnt(0)"
            : "=&v"(yv[0]), "=&v"(yv[1]), "=&v"(yv[2]), "=&v"(yv[3]), "=&v"(yv[4]), "=&v"(yv[5]), "=&v"(yv[6]), "=&v"(yv[7])
            : "v"(go), "s"(ws + WS_YT), "v"(go + 4096u), "v"(go + 8192u), "v"(go + 12288u), "s"(yb)
            : "memory");
        if (!YSP) { CS(13) }
        const unsigned oo = (unsigned)((rb * 128 + lw * 32 + r8 * 4) * 4096 + 2048 + (cb * 64 + cp * 8) * 2);
#pragma unroll
        for (int i = 0; i < 4; i++) {
          const unsigned sel = (i & 1) ? 0x07060302u : 0x05040100u;
          u32x4 v;
#pragma unroll
          for (int q = 0; q < 4; q++) v[q] = __builtin_amdgcn_perm(yv[2 * q + 1][i >> 1], yv[2 * q][i >> 1], sel);
          st16_sc1<0, ONT>((char*)out + i * 4096, oo, v);
        }
        }
      }
      }
      STAMP(4);
    }
    asm volatile(".Lkend2:");
    return;
  }

  KSTAMP(13)
  SB();
  TADR(0)
  GLT(0)
  SB();
  KSTAMP(14)
  SB();
  KSTAMP(15)
  SB();
  if (tid < 64) lflag[tid] = 0u;
  unsigned aaddr[2];
#pragma unroll
  for (int k2 = 0; k2 < 2; k2++) aaddr[k2] = (wm * 64 + i16) * 128 + (((4 * k2 + g) ^ ((i16 >> 1) & 7)) * 16);
  const int fsw = ((i16 >> 3) & 1) | ((g & 1) << 1);
  unsigned bad[4];
#pragma unroll
  for (int n = 0; n < 4; n++)
    bad[n] = WOFF + (n >> 1) * 8192 + (8 * g + (i16 >> 2)) * 128 + (((wn * 2 + (n < 2 ? n : (n & 1) ^ wm)) ^ fsw) * 32) + (i16 & 3) * 8;
  asm volatile("" : "+v"(bad[0]), "+v"(bad[1]), "+v"(bad[2]), "+v"(bad[3]));
  f32x4 acc[4][4];
  f32x4 eps4 = {1e-6f, 1e-6f, 1e-6f, 1e-6f};
  asm volatile("" : "+v"(eps4));
  bf16x8 fa[2][8], R[3][8];
  s16x4 fbl[2][6], fbh[2][6];
  f32x4 aq[4][2];
#define AQINIT _Pragma("unroll") for (int i = 0; i < 4; i++) { aq[i][0] = f32x4{1e-6f, 1e-6f, 1e-6f, 1e-6f}; aq[i][1] = aq[i][0]; asm volatile("" : "+v"(aq[i][0]), "+v"(aq[i][1])); }
  unsigned bq[2] = {bad[0] + (QF - WOFF), bad[1] + (QF - WOFF)};
  asm volatile("" : "+v"(bq[0]), "+v"(bq[1]));
#define RDA(F, mt, st, k2) F[mt] = *(const LAS bf16x8*)(lds + aaddr[k2] + (st) * STG + (mt) * 2048)
#define RDBL(buf, n, p) fbl[buf][n] = __builtin_amdgcn_ds_read_tr16_b64_v4i16((LAS s16x4*)(p))
#define RDBH(buf, n, p) fbh[buf][n] = __builtin_amdgcn_ds_read_tr16_b64_v4i16((LAS s16x4*)((p) + 512))
#define FB(buf, n) BC8(__builtin_shufflevector(fbl[buf][n], fbh[buf][n], 0, 1, 2, 3, 4, 5, 6, 7))
#define RD4(F, c, j, nst, nk2)                                               \
  if ((j) == 0) RDA(F, 0, nst, nk2);                                         \
  else if ((j) <= 8) {                                                       \
    if (((j) - 1) & 1) RDBH(c, ((j) - 1) >> 1, lds + bad[((j) - 1) >> 1] + (nst) * STG + (nk2) * 4096); \
    else RDBL(c, ((j) - 1) >> 1, lds + bad[((j) - 1) >> 1] + (nst) * STG + (nk2) * 4096);               \
  } else if ((j) < 12) RDA(F, (j) - 8, nst, nk2);
#define RDQ(c, j, xs, qs)                                                                        \
  if (((j) & 7) == 2 || ((j) & 7) >= 5) {                                                        \
    if ((xs) >= 0) RDA((fa[c] + ((j) >> 3) * 4), ((j) & 7) == 2 ? 0 : ((j) & 7) - 4, (xs) < 0 ? 0 : (xs), (j) >> 3); \
  } else {                                                                                       \
    const int n_ = ((j) & 7) >= 3 ? 1 : 0, h_ = ((j) & 7) == 1 || ((j) & 7) == 4;                \
    if (h_) RDBH(c, ((j) >> 3) * 2 + n_, lds + bad[n_] + (qs) * 8192 + ((j) >> 3) * 4096);       \
    else RDBL(c, ((j) >> 3) * 2 + n_, lds + bad[n_] + (qs) * 8192 + ((j) >> 3) * 4096);          \
  }
#define STEP4(t, FC, FN, c, RD, nst, nk2, BAR)                                     \
  _Pragma("unroll") for (int q = 0; q < 16; q++) {                                 \
    const int mt = q / 4, n = q % 4;                                               \
    if (q == 0 && (BAR)) { CPRE(t) BARRIER(); SB(); }                              \
    acc[mt][n] = MFMA16(FC[mt], FB(c, n), acc[mt][n]);                             \
    if (RD) { RD4(FN, c ^ 1, q, nst, nk2) }                                        \
    if ((t) == 15 && (BAR)) { RDQ(0, q, -1, 2) }                                   \
    CEV(t, BAR, q)                                                                 \
    SB();                                                                          \
  }
#define RD6(F, c, j, nst, nk2)                                               \
  if ((j) >= 1 && (j) <= 4) {                                                \
    if (((j) - 1) & 1) RDBH(c, 4 + (((j) - 1) >> 1), lds + bq[((j) - 1) >> 1] + (nst) * 8192 + (nk2) * 4096); \
    else RDBL(c, 4 + (((j) - 1) >> 1), lds + bq[((j) - 1) >> 1] + (nst) * 8192 + (nk2) * 4096);               \
  } else { RD4(F, c, ((j) ? (j) - 4 : 0), nst, nk2) }
#define STEP6(t, FC, FN, c, NFULL, nst, nk2, BAR)                                  \
  _Pragma("unroll") for (int q = 0; q < 24; q++) {                                 \
    const int mt = q / 6, n = q % 6;                                               \
    if (q == 0 && (BAR)) { CPRE(t) BARRIER(); SB(); }                              \
    if ((t) == 0 && !(BAR)) {                                                      \
      if (n < 2) aq[mt][n] = eps4;                                                 \
      else acc[mt][n - 2] = n < 4 ? eps4 : f32x4{0, 0, 0, 0};                      \
    }                                                                              \
    if (n < 2) aq[mt][n] = MFMA16(FB(c, 4 + n), FC[mt], aq[mt][n]);                \
    else acc[mt][n - 2] = MFMA16(FC[mt], FB(c, n - 2), acc[mt][n - 2]);            \
    if (NFULL) { RD6(FN, c ^ 1, q, nst, nk2) } else { RD4(FN, c ^ 1, q, nst, nk2) } \
    CEV(t, BAR, q)                                                                 \
    SB();                                                                          \
  }
#define TILE6(t, st, nst, NFULL, FN) STEP6(t, fa[0], fa[1], 0, true, st, 1, 0) STEP6(t, fa[1], FN, 1, NFULL, nst, 0, 1)
#define TILE4(t, F0, F1, F2, st, nst, RD)  \
  STEP4(t, F0, F1, 0, true, st, 1, 0)      \
  STEP4(t, F1, F2, 1, RD, nst, 0, 1)

  asm volatile("" : "+v"(aaddr[0]), "+v"(aaddr[1]));
  SB();
  u32x2 k0;
  {
    TABADR(ctab)
    KSTAMP(9)
    PMOV(NF > 0 ? 11 : 9)
    KSTAMP(10)
    KVARS
    SB(); dt = rbf(__builtin_amdgcn_exp2f(lo2f(p_ldt) * 1.44269504f)); SB();
    SB(); SETA(0) SB();
    SB(); SETA(1) GLF(1, 0) SB();
    SB(); CEXA(0, 1.f) SB();
    SB(); CEXB(z, 0, 1.f) SB();
    SB(); CEXA(1, 1.f) GLF(1, 1) SB();
    SB(); CEXB(z, 1, 1.f) SB();
    SB(); SETC(0) SB();
    SB(); SETC(1) e[0] = c[0]; e[1] = c[1]; GLA(1, 0) SB();
    SB(); PK(T[0], 0, e) PK(T[0], 1, e) asm volatile("" : "+v"(T[0][0])); asm volatile("" : "+v"(T[0][1])); SB();
    SB(); ADVP(e, e, z, T[1]) W2(0, T[0], 1, T[1]) SB();
    SB(); ADVP(e, e, z, T[2]) GLA(1, 1) SB();
    SB(); ADVP(e, e, z, T[3]) W2(2, T[2], 3, T[3]) SB();
    SB(); ADVP(e, e, z, T[4]) SB();
    SB(); ADVP(e, e, z, T[5]) W2(4, T[4], 5, T[5]) GLA(1, 2) SB();
    SB(); ADVP(e, e, z, T[6]) SB();
    SB(); ADVP(e, e, z, T[7]) W2(6, T[6], 7, T[7]) SB();
    SB(); ADVP(e, e, z, T[8]) GLA(1, 3) SB();
    SB(); ADVP(e, e, z, T[9]) W2(8, T[8], 9, T[9]) SB();
    SB(); ADVP(e, e, z, T[10]) SB();
    SB(); ADVP(e, e, z, T[11]) W2(10, T[10], 11, T[11]) GLB(1, 0) SB();
    SB(); ADVP(e, e, z, T[12]) SB();
    SB(); ADVP(e, e, z, T[13]) W2(12, T[12], 13, T[13]) SB();
    SB(); ADVP(e, e, z, T[14]) GLB(1, 1) SB();
    SB(); ADVP(e, e, z, T[15]) W2(14, T[14], 15, T[15]) SB();
    SB(); CEXA(0, 16.f) GRD(0, 0) GRD(0, 1) GRD(0, 2) GRD(0, 3) SB();
    SB(); CEXB(z, 0, -1.f) GRD(0, 4) GRD(0, 5) GRD(0, 6) GRD(0, 7) GLB(1, 2) SB();
    SB(); CEXA(1, 16.f) SB();
    SB(); CEXB(z, 1, -1.f) e[0] = z[0]; e[1] = z[1]; SB();
    SB(); PK(T[16], 0, e) PK(T[16], 1, e) asm volatile("" : "+v"(T[16][0])); asm volatile("" : "+v"(T[16][1])); W2(0, (u32x2{0x00003f80u, 0x00003f80u}), 1, T[16]) GLB(1, 3) SB();
    SB(); ADVP(e, e, z, T[17]) SB();
    SB(); ADVP(e, e, z, T[18]) W2(2, T[17], 3, T[18]) SB();
    SB(); ADVP(e, e, z, T[19]) SB();
    SB(); ADVP(e, e, z, T[20]) W2(4, T[19], 5, T[20]) SB();
    SB(); ADVP(e, e, z, T[21]) SB();
    SB(); ADVP(e, e, z, T[22]) W2(6, T[21], 7, T[22]) SB();
    SB(); ADVP(e, e, z, T[23]) SB();
    SB(); ADVP(e, e, z, T[24]) W2(8, T[23], 9, T[24]) SB();
    SB(); ADVP(e, e, z, T[25]) SB();
    SB(); ADVP(e, e, z, T[26]) W2(10, T[25], 11, T[26]) SB();
    SB(); ADVP(e, e, z, T[27]) SB();
    SB(); ADVP(e, e, z, T[28]) W2(12, T[27], 13, T[28]) SB();
    SB(); ADVP(e, e, z, T[29]) SB();
    SB(); ADVP(e, e, z, T[30]) W2(14, T[29], 15, T[30]) SB();
    SB();  SB();
    KSTAMP(11) ERD(0) ERD(1) ERD(2) ERD(3) ERD(4) ERD(5) ERD(6) ERD(7) SB(); BARP SB();
    MF(0, 0) MF(0, 1) MF(0, 2) MF(0, 3) MF(0, 4) MF(0, 5) MF(0, 6) MF(0, 7)
    k0 = u32x2{pack2(vf1(D[0][3]), D[0][2]), pack2(D[0][1], D[0][0] + (lane ? 0.f : lo2f(p_dsk)))};
    asm volatile("" : "+v"(k0));
  }
  STAMP(12);
  SB();
  WAIT_VM(NF > 1 ? 10 : 8);
  BARRIER();
  PROBE(1)
  STAMP(1);
#pragma unroll
  for (int j = 0; j < 16; j++) { if (NF) { RD6(fa[0], 0, j, 0, 0) } else { RD4(fa[0], 0, j, 0, 0) } }
  SB();
  TILE6(0, 0, 1, true, fa[0])
  TILE6(1, 1, 2, true, fa[0])
  TILE6(2, 2, 0, true, fa[0])
  TILE6(3, 0, 1, true, fa[0])
  TILE6(4, 1, 2, true, fa[0])
  TILE6(5, 2, 0, true, fa[0])
  TILE6(6, 0, 1, true, fa[0])
  TILE6(7, 1, 2, true, fa[0])
  TILE6(8, 2, 0, true, fa[0])
  TILE6(9, 0, 1, false, fa[0])
  TILE4(10, fa[0], fa[1], fa[0], 1, 2, true)
  TILE4(11, fa[0], fa[1], fa[0], 2, 0, true)
  TILE4(12, fa[0], fa[1], R[0], 0, 1, true)
  TILE4(13, R[0], (R[0] + 4), R[1], 1, 2, true)
  TILE4(14, R[1], (R[1] + 4), R[2], 2, 0, true)
  TILE4(15, R[2], (R[2] + 4), R[2], 0, 1, false)
  SB();
  PROBE(2)
#define EPRIO 2
  __builtin_amdgcn_s_setprio(EPRIO);
  STAMP(2);
  {
    f32x4 kv[2][2], zz[2];
#pragma unroll
    for (int a = 0; a < 2; a++) { kv[a][0] = f32x4{0, 0, 0, 0}; kv[a][1] = f32x4{0, 0, 0, 0}; zz[a] = f32x4{0, 0, 0, 0}; }
    const u32x4 ones = vv4(0x3f803f80u);
#define QM(k)                                                                                        \
    if ((k) < 32) {                                                                                  \
      const int s_ = ((k) >> 4) & 1, q_ = (k) & 15, k2 = q_ >> 3, mt = (q_ >> 1) & 3, n = q_ & 1;    \
      SB();                                                                                          \
      aq[mt][n] = MFMA16(FB(s_, k2 * 2 + n), R[s_][k2 * 4 + mt], aq[mt][n]);                         \
      if ((k) >= QR0 && (k) < QR0 + 8) { RDQ(1, 2 * ((k) - QR0), -1, 3) RDQ(1, 2 * ((k) - QR0) + 1, -1, 3) } \
      SB();                                                                                          \
    }
#define QE2 0
#pragma unroll
    for (int p = 0; p < 2; p++) {
      u32x4 vf[2];
#pragma unroll
      for (int et = 0; et < 2; et++) {
        vf[et][0] = pack2(acc[2 * p][2 + et][0], acc[2 * p][2 + et][1]); vf[et][1] = pack2(acc[2 * p][2 + et][2], acc[2 * p][2 + et][3]);
        asm volatile("" : "+v"(vf[et][0]), "+v"(vf[et][1]));
        vf[et][2] = pack2(acc[2 * p + 1][2 + et][0], acc[2 * p + 1][2 + et][1]); vf[et][3] = pack2(acc[2 * p + 1][2 + et][2], acc[2 * p + 1][2 + et][3]);
        asm volatile("" : "+v"(vf[et][2]), "+v"(vf[et][3]));
      }
#pragma unroll
      for (int dt = 0; dt < 2; dt++) {
        u32x4 pk;
#pragma unroll
        for (int j = 0; j < 4; j++) {
          pk[j] = phi2(acc[2 * p + (j >> 1)][dt][(j & 1) * 2], acc[2 * p + (j >> 1)][dt][(j & 1) * 2 + 1]);
          asm volatile("" : "+v"(pk[j]));
        }
        SB();
        kv[dt][1] = MFMA16(BC8(pk), BC8(vf[1]), kv[dt][1]);
        zz[dt] = MFMA16(BC8(pk), BC8(ones), zz[dt]);
        kv[dt][0] = MFMA16(BC8(pk), BC8(vf[0]), kv[dt][0]);
        SB();
      }
    }
    *(LAS f32x4*)(lds + XCH + (wid ^ 1) * 2048 + l16) = kv[0][1];
    *(LAS f32x4*)(lds + XCH + (wid ^ 1) * 2048 + 1024 + l16) = kv[1][1];
    if (wm == 1 && i16 == 0) {
      *(LAS f32x4*)(lds + MISC + 256 + wn * 128 + g * 32) = zz[0];
      *(LAS f32x4*)(lds + MISC + 256 + wn * 128 + g * 32 + 16) = zz[1];
    }
    asm volatile("" ::: "memory");
    if (lane == 0) lflag[4 + wid] = 1u;
#pragma unroll
    for (int k = 0; k < QE; k++) { QM(k) }
    lds_wait(lflag + 4 + (wid ^ 1));
    asm volatile("" ::: "memory");
    f32x4 keep0 = *(const LAS f32x4*)(lds + XCH + wid * 2048 + l16);
    f32x4 keep1 = *(const LAS f32x4*)(lds + XCH + wid * 2048 + 1024 + l16);
#pragma unroll
    for (int k = QE; k < QE + QE2; k++) { QM(k) }
    keep0 += kv[0][0]; keep1 += kv[1][0];
    auto a = __builtin_amdgcn_permlane16_swap(min(pack2(keep0[0], keep0[1]), SENT - 1), min(pack2(keep1[0], keep1[1]), SENT - 1), false, false);
    auto b = __builtin_amdgcn_permlane16_swap(min(pack2(keep0[2], keep0[3]), SENT - 1), min(pack2(keep1[2], keep1[3]), SENT - 1), false, false);
    CPRE(16)
    st16_sc1<0>(kv_out, l16, u32x4{a[0], b[0], a[1], b[1]});
    if (wm == 0 && i16 == 0) {
      f32x4 z0 = zz[0] + *(const LAS f32x4*)(lds + MISC + 256 + wn * 128 + g * 32);
      f32x4 z1 = zz[1] + *(const LAS f32x4*)(lds + MISC + 256 + wn * 128 + g * 32 + 16);
      u32x4 t0 = __builtin_bit_cast(u32x4, z0), t1 = __builtin_bit_cast(u32x4, z1);
#pragma unroll
      for (int j = 0; j < 4; j++) { t0[j] = min(t0[j], SENT - 1); t1[j] = min(t1[j], SENT - 1); }
      st16_sc1<0>(z_out, (unsigned)g * 32u, t0);
      st16_sc1<16>(z_out, (unsigned)g * 32u, t1);
    }
    HINT(WS_H1)
#pragma unroll
    for (int k = QE + QE2; k < 16; k++) { QM(k) }
  }
  SB();
  STAMP(3);
  u32x4 pq[4], kvf, zf;
  WOXR
  {
    const char* kv_in = ws + WS_KVP + (size_t)(((cb * 2 + wn) * 2 + wm) * 16) * 1024 + 4096;
    const char* z_in = ws + WS_Z + (size_t)((cb * 2 + wn) * 16) * 128;
    u32x4 pv[16], zv[2];
    u32x2 flg;
    unsigned pend = 0x1113u;
    const unsigned own = 0x10001u << rb;
    int spins = 0;
#define XADR RELANE const unsigned zo = (unsigned)(i16 * 128 + g * 32), po = (unsigned)(g * 1024 + i16 * 16), \
                                   ho = (unsigned)((cg_of(i16, cb) * 4 + ((g & 1) ? (wid & 2) : wid)) * 128); (void)zo; (void)po; (void)ho;
#define XHINT { XADR asm volatile("global_load_dwordx2 %0, %1, %2 sc1" : "=v"(flg) : "v"(ho), "s"(ws + WS_H1) : "memory"); \
                SB(); }
#define XDEF { _Pragma("unroll") for (int p = 0; p < 16; p++) asm volatile("" : "=v"(pv[p])); asm volatile("" : "=v"(zv[0]), "=v"(zv[1])); }
#define PL(p, b, off) "global_load_dwordx4 %" #p ", %20, %" #b " offset:" #off " sc1\n\t"
#define PG(bit, l) "s_bitcmp1_b32 %19, " #bit "\n\ts_cbranch_scc0 1f\n\t" l "1:\n\t"
#define PVS "+v"(pv[0]), "+v"(pv[1]), "+v"(pv[2]), "+v"(pv[3]), "+v"(pv[4]), "+v"(pv[5]), "+v"(pv[6]), "+v"(pv[7]), \
            "+v"(pv[8]), "+v"(pv[9]), "+v"(pv[10]), "+v"(pv[11]), "+v"(pv[12]), "+v"(pv[13]), "+v"(pv[14]), "+v"(pv[15]), "+v"(zv[0]), "+v"(zv[1]), "+v"(flg)
#define XLOAD(nw)                                                                                                       \
    asm volatile(PG(2, "global_load_dwordx2 %18, %25, %26 sc1\n\t")                                                     \
                 PG(1, "global_load_dwordx4 %16, %23, %24 sc1\n\tglobal_load_dwordx4 %17, %23, %24 offset:16 sc1\n\t")  \
                 PG(0, PL(0, 21, -4096) PL(1, 21, -3840) PL(2, 21, -3584) PL(3, 21, -3328))                             \
                 PG(4, PL(4, 21, 0) PL(5, 21, 256) PL(6, 21, 512) PL(7, 21, 768))                                       \
                 PG(8, PL(8, 22, -4096) PL(9, 22, -3840) PL(10, 22, -3584) PL(11, 22, -3328))                           \
                 PG(12, PL(12, 22, 0) PL(13, 22, 256) PL(14, 22, 512) PL(15, 22, 768))                                  \
                 : PVS                                                                                                  \
                 : "s"(nw), "v"(po), "s"(kv_in), "s"(kv_in + 8192), "v"(zo), "s"(z_in), "v"(ho), "s"(ws + WS_H1)        \
                 : "memory", "scc");
#define XES(i)
#define XSTEP(force)                                                                                                    \
  {                                                                                                                     \
    XADR                                                                                                                \
    asm volatile("s_waitcnt vmcnt(0)" : PVS::"memory");                                                                 \
    const unsigned rdy = (unsigned)__builtin_amdgcn_ballot_w64(flg[0] == tag[0] && flg[1] == tag[1]) | own;             \
    unsigned a_ = rdy & (rdy >> 1);                                                                                     \
    a_ &= a_ >> 2;                                                                                                      \
    unsigned nw = ((a_ & 0x1111u) | ((rdy >> 16) == 0xFFFFu ? 2u : 0u) | ((force) ? 0x1113u : 0u)) & pend;              \
    pend &= ~nw;                                                                                                        \
    nw |= pend ? 4u : 0u;                                                                                               \
    asm volatile("" : "+s"(nw));                                                                                        \
    XLOAD(nw)                                                                                                           \
    SB();                                                                                                               \
  }
#define TILEQ(s, FC, BAR, xs, qs)                                                  \
  XPT(s)                                                                           \
  _Pragma("unroll") for (int q = 0; q < 16; q++) {                                 \
    const int k2 = q >> 3, mt = (q >> 1) & 3, n = q & 1;                           \
    if (q == 0 && (BAR)) { BARRIER(); SB(); }                                      \
    aq[mt][n] = MFMA16(FB((s) & 1, k2 * 2 + n), FC[k2 * 4 + mt], aq[mt][n]);       \
    if ((qs) >= 0 && q < 8) { RDQ(((s) + 1) & 1, 2 * q, xs, qs) RDQ(((s) + 1) & 1, 2 * q + 1, xs, qs) } \
    CEV(16 + (s), 0, q)                                                            \
    SB();                                                                          \
  }
#define XA (NSQ - 1)
#define XPT(s) if ((s) == XA) XHINT
#define QPRIO 0
    __builtin_amdgcn_s_setprio(QPRIO);
    SB();
    if (F16) lds_wait_all(MISC + (fresh_lane() & 3) * 4, 176); else BARRIER();
    STAMP(13);
    SB();
    TILEQ(1, R[1], 0, 1, 4)
    TILEQ(2, fa[0], 0, 2, 5)
    TILEQ(3, fa[1], 0, 0, 0)
    TILEQ(4, fa[0], 0, -1, 1)
    TILEQ(5, R[2], 0, -1, -1)
    STAMP(9);
    __builtin_amdgcn_s_setprio(2);
    STAMP(14);
    SB();
#define PHI(e) pq[(e) >> 2][(e) & 3] = phi2(aq[(e) >> 2][((e) >> 1) & 1][((e) & 1) * 2], aq[(e) >> 2][((e) >> 1) & 1][((e) & 1) * 2 + 1]); \
               asm volatile("" : "+v"(pq[(e) >> 2][(e) & 3]));
#pragma unroll
    for (int e = 0; e < PH0; e++) { PHI(e) }
    RELANE
    const unsigned one = (i16 & 1) ? 0x3f800000u : 0x00003f80u;
    u32x4 sl[2];
#pragma unroll
    for (int j = 0; j < 4; j++) { sl[0][j] = (i16 >> 1) == j ? one : 0u; sl[1][j] = (i16 >> 1) == 4 + j ? one : 0u; }
    asm volatile("" : "+v"(sl[0]), "+v"(sl[1]));
    f32x4 sk[2], sz[2];
#define XM(p) sk[(p) & 1] = MFMA16(BC8(sl[((p) >> 1) & 1]), BC8(pv[p]), ((p) < 2 ? f32x4{0, 0, 0, 0} : sk[(p) & 1]));
#define XD(k) sz[(k) & 1][((k) >> 1) & 3] = dppadd((k) < 8 ? __builtin_bit_cast(f32x4, zv[(k) & 1])[((k) >> 1) & 3] : sz[(k) & 1][((k) >> 1) & 3], (k) >> 3);
#define XRED                                                   \
    _Pragma("unroll") for (int p = 0; p < 16; p++) { XM(p) }   \
    _Pragma("unroll") for (int k = 0; k < 32; k++) { XD(k) }
#define LOWP __builtin_amdgcn_s_setprio(0);
#define HIGHP __builtin_amdgcn_s_setprio(2);
#define XWZ(n) asm volatile("s_waitcnt vmcnt(%2)" : "+v"(zv[0]), "+v"(zv[1]) : "n"(n) : "memory");
#define XWG(G, n) asm volatile("s_waitcnt vmcnt(%4)" : "+v"(pv[4 * (G)]), "+v"(pv[4 * (G) + 1]), "+v"(pv[4 * (G) + 2]), "+v"(pv[4 * (G) + 3]) : "n"(n) : "memory");
#define XL(p) ld16_sc1<(((p) >> 2) & 1) * 4096 - 4096 + ((p) & 3) * 256>(pv[p], kv_in + ((p) >> 3) * 8192, po);
#define XZ(k) ld16_sc1<(k) * 16>(zv[k], z_in, zo);
    if (WPH) lds_wait_all(MISC + (lane & 7) * 4, 192);
    SB();
    {
      XADR
      asm volatile("s_waitcnt vmcnt(0)" : "+v"(flg)::"memory");
      XES(2)
      if (__builtin_expect(((unsigned)__builtin_amdgcn_ballot_w64(flg[0] == tag[0] && flg[1] == tag[1]) | own) == 0xFFFFFFFFu, 1)) {
        XZ(0) XZ(1) XL(0) SB();
        PHI(3) SB();
        XL(1) XL(2) XL(3) SB();
        PHI(4) SB();
        XL(4) XL(5) XL(6) SB();
        PHI(5) SB();
        XL(7) XL(8) XL(9) SB();
        PHI(6) SB();
        XL(10) XL(11) XL(12) SB();
        PHI(7) SB();
        XL(13) XL(14) XL(15) SB();
        XES(13)
        PHI(8) SB();
        PHI(9) SB();
        XWZ(16) XWG(0, 12) XM(0) XM(1) XD(0) XD(1) XD(2) XD(3) PHI(10) SB();
        XM(2) XM(3) XD(4) XD(5) XD(6) XD(7) PHI(11) SB();
        XWG(1, 8) XM(4) XM(5) XD(8) XD(9) XD(10) XD(11) PHI(12) SB();
        XM(6) XM(7) XD(12) XD(13) XD(14) XD(15) PHI(13) SB();
        XWG(2, 4) XM(8) XM(9) XD(16) XD(17) XD(18) XD(19) PHI(14) SB();
        XM(10) XM(11) XD(20) XD(21) XD(22) XD(23) PHI(15) XES(12) SB();
        XWG(3, 0) XM(12) XM(13) XD(24) XD(25) XD(26) XD(27) SB();
        XM(14) XM(15) XD(28) XD(29) XD(30) XD(31) SB();
        XES(7)
      } else {
        XDEF
        XSTEP(false)
#pragma unroll
        for (int e = PH0; e < 16; e++) { PHI(e) }
        SB();
        while (pend) { ++spins; XSTEP(spins > SPIN_CAP) }
        WO_P
        asm volatile("s_waitcnt vmcnt(%19)" : PVS : "n"(WPH) : "memory");
        XES(7)
        XRED
      }
    }
    STAMP(10);
    const float chk = ((sz[0][0] + sz[0][1]) + (sz[0][2] + sz[0][3])) + ((sz[1][0] + sz[1][1]) + (sz[1][2] + sz[1][3])) + (sk[0][0] + sk[1][0]);
    if (__builtin_expect(__builtin_amdgcn_ballot_w64(chk != chk) != 0ull, 0)) {
      asm volatile("" : "=v"(flg));
      while (true) {
        XADR
        XLOAD(0x1113u)
        asm volatile("s_waitcnt vmcnt(0)" : PVS::"memory");
        unsigned mx = max(max4(zv[0]), max4(zv[1]));
#pragma unroll
        for (int p = 0; p < 16; p++) mx = max(mx, max4(pv[p]));
        spins += 100;
        if (__builtin_amdgcn_ballot_w64(mx == SENT) == 0ull || spins > 100 * SPIN_CAP) break;
      }
      XRED
    }
    kvf = u32x4{pack2(sk[0][0], sk[0][1]), pack2(sk[0][2], sk[0][3]), pack2(sk[1][0], sk[1][1]), pack2(sk[1][2], sk[1][3])};
    *(LAS u32x4*)(lds + XCH + wid * 2048 + l16) = kvf;
    asm volatile("" ::: "memory");
    QFL(0)
    zf = u32x4{pack2(sz[0][0], sz[0][1]), pack2(sz[0][2], sz[0][3]), pack2(sz[1][0], sz[1][1]), pack2(sz[1][2], sz[1][3])};
    STAMP(11);
  }
  {
    RELANE
    const f32x4 zero = {0, 0, 0, 0};
    float rd[4];
    f32x4 dn[4], n[4];
    u32x4 v[4], kvo;
#define OMF(d, F) _Pragma("unroll") for (int mt = 0; mt < 4; mt++) { d[mt] = MFMA16(BC8(F), BC8(pq[mt]), zero); SB(); }
#define OSC(h, mt) v[mt][2 * (h)] = min(pack2(n[mt][0] * rd[mt], n[mt][1] * rd[mt]), SENT - 1); v[mt][2 * (h) + 1] = min(pack2(n[mt][2] * rd[mt], n[mt][3] * rd[mt]), SENT - 1);
#define OST(W)                                                                                                        \
    OSC(W, 0) OSC(W, 1) OSC(W, 2) OSC(W, 3)                                                                           \
    asm volatile("" : "+v"(v[0][2 * (W)]), "+v"(v[1][2 * (W)]), "+v"(v[2][2 * (W)]), "+v"(v[3][2 * (W)]),             \
                      "+v"(v[0][2 * (W) + 1]), "+v"(v[1][2 * (W) + 1]), "+v"(v[2][2 * (W) + 1]), "+v"(v[3][2 * (W) + 1])); \
    SB();                                                                                                             \
    lds_get16(kvo, ga, gb);                                                                                           \
    SB();                                                                                                             \
    OMF(n, kvo)                                                                                                       \
    QFL(28)                                                                                                           \
    STAMP(4);                                                                                                         \
    SB();                                                                                                             \
    OSC(1 - (W), 0) st16_sc1<0>(oc, l16, v[0]); SB(); OSC(1 - (W), 1) st16_sc1<1024>(oc, l16, v[1]); SB();           \
    OSC(1 - (W), 2) st16_sc1<2048>(oc, l16, v[2]); SB(); OSC(1 - (W), 3) st16_sc1<3072>(oc, l16, v[3]);
    SB();
    OMF(dn, zf)
    OMF(n, kvf)
    unsigned ga = MISC + (wid ^ 1) * 4, gb = XCH + (wid ^ 1) * 2048 + l16;
    asm volatile("" : "+v"(ga), "+v"(gb));
#pragma unroll
    for (int mt = 0; mt < 4; mt++) rd[mt] = __builtin_amdgcn_rcpf(dn[mt][0] + 1e-6f);
    asm volatile("" : "+v"(rd[0]), "+v"(rd[1]), "+v"(rd[2]), "+v"(rd[3]), "+v"(n[0]), "+v"(n[1]), "+v"(n[2]), "+v"(n[3]));
    if (wm) { OST(1) } else { OST(0) }
    HINT(WS_H2)
  }
  SB();
  STAMP(5);
  {
    RELANE2
    const int kh = wn;
    const int fsw = ((i16 >> 3) & 1) | ((g & 1) << 1);
    const char* o_in = ws + WS_O + (size_t)(((rb2 * 32 + kh * 16) * 8 + wm * 4) * 1024);
    unsigned wad[4];
#pragma unroll
    for (int nt = 0; nt < 4; nt++) wad[nt] = (unsigned)(kh * KH + (8 * g + (i16 >> 2)) * 128 + ((nt ^ fsw) * 32) + (i16 & 3) * 8);
    const int hh = (i16 + rot) & 15;
    unsigned po = (unsigned)(WS_H2 + (cg_of(rb2, kh * 8 + (hh >> 1)) * 4 + (hh & 1) * 2 + wm) * 128);
    u32x2 flg;
#define OPOLL asm volatile("global_load_dwordx2 %0, %1, %2 sc1" : "=v"(flg) : "v"(po), "s"(ws) : "memory");
#define XFREE lds_wait4(lflag + 28);
    WO_R
    STAMP(15);
    asm volatile("s_waitcnt vmcnt(%1)" : "+v"(flg) : "n"(WN2) : "memory");
    bool go = !FLAGBAD;
    f32x4 a2[4][4];
    const unsigned lk = l16 + kh * 2048, lg = lk ^ 2048u;
    int tries = 0;
    unsigned wtab = (unsigned)(i16 * 4096);
    wtab = i16 >= 12 ? SS + (i16 - 12) * SCH + 4608 + kh * (2 * SCH - KH) : wtab;
    wtab = i16 == 14 ? XCH + kh * (4096 - KH) : wtab;
    wtab = i16 == 15 ? SS + kh * (2 * SCH - KH) : wtab;
    asm volatile("" : "+v"(wtab), "+v"(po));
    unsigned sp = 0;
    const unsigned fadr = MISC;
    while (true) {
      {
        int spins = 0;
        while (!go) {
          asm volatile("global_load_dwordx2 %0, %1, %2 sc1\n\ts_waitcnt vmcnt(0)" : "=v"(flg) : "v"(po), "s"(ws) : "memory");
#define EWF 1
          if (EWF) QFL(36)
          go = !FLAGBAD || ++spins > SPIN_CAP;
          if (!go) __builtin_amdgcn_s_sleep(GS1);
        }
      }
      STAMP(6);
      u32x4 of[8][4];
      const char* op;
#define PICK(i) { int ri = rot; asm volatile("" : "+s"(ri)); op = o_in + (((i) + ri) & 15) * 8192; }
#define PADR(i) asm volatile("" : "+s"(op));
#define OLD(i, rt) if ((rt) & 1) ld16_sc1<1024>(of[(i) & 7][rt], op, (rt) < 2 ? lk : lg); else ld16_sc1<0>(of[(i) & 7][rt], op, (rt) < 2 ? lk : lg);
#pragma unroll
      for (int i = 0; i < G2PD; i++) { PICK(i) PADR(i) OLD(i, 0) OLD(i, 1) OLD(i, 2) OLD(i, 3) }
      PICK(G2PD) PADR(G2PD)
      WAIT_VM(4 * G2PD);
      QFL(36)
      s16x4 wl[2][4], wh[2][4];
      unsigned so;
#define WNEED(i)                                                                    \
  if ((i) == 0) lds_wait_all(fadr + (lane & 7) * 4, 128);
#define WREG(i) (DYN == 2 && (i) < 12)
#define WSO(i) if (!WREG(i)) { so = (unsigned)__builtin_amdgcn_readlane((int)wtab, (i)); so -= sp; sp += so; }
#define WAD(i, nt) if (!WREG(i)) { wad[nt] += so; asm volatile("" : "+v"(wad[nt])); }
#define WRL(i, nt) wl[(i) & 1][nt] = __builtin_amdgcn_ds_read_tr16_b64_v4i16((LAS s16x4*)(size_t)(wad[nt] + (WREG(i) ? (i) * 4096 : 0)));
#define WRH(i, nt) wh[(i) & 1][nt] = __builtin_amdgcn_ds_read_tr16_b64_v4i16((LAS s16x4*)(size_t)(wad[nt] + (WREG(i) ? (i) * 4096 : 0) + 512));
      if (DYN == 2) {
#pragma unroll
        for (int nt = 0; nt < 4; nt++) wad[nt] -= sp;
        sp = 0;
      }
      WNEED(0)
      WSO(0)
#pragma unroll
      for (int nt = 0; nt < 4; nt++) { WAD(0, nt) WRL(0, nt) WRH(0, nt) }
#define OW(i, rt, n) asm volatile("s_waitcnt vmcnt(%1)" : "+v"(of[(i) & 7][rt]) : "n"(n) : "memory");
#define GSTP(i, q)
#define G2S(i, q, PRE, POST)                                                     \
  {                                                                              \
    const int rt = q >> 2, nt = q & 3;                                           \
    SB();                                                                        \
    if (q == 0 && i == YR) QFL(16)                                               \
    if (YP == 2 && q == 0 && i == YQ) QFL(8)                                     \
    if (q == 0 && i == G2R) __builtin_amdgcn_s_setprio(2);                       \
    PRE                                                                          \
    a2[rt][nt] = MFMA16(BC8(__builtin_shufflevector(wl[i & 1][nt], wh[i & 1][nt], 0, 1, 2, 3, 4, 5, 6, 7)), BC8(of[i & 7][rt]), (i ? a2[rt][nt] : f32x4{0, 0, 0, 0})); \
    SB();                                                                        \
    GSTP(i, q)                                                                   \
    if (i < 15) {                                                                \
      if (q == 0) { WSO(i + 1) }                                                 \
      if (q == 2) { WAD(i + 1, 0) WAD(i + 1, 1) }                                \
      if (q == 3) { WRL(i + 1, 0) }                                              \
      if (q == 4) { WRH(i + 1, 0) WRL(i + 1, 1) }                                \
      if (q == 5) { WRH(i + 1, 1) WAD(i + 1, 2) }                                \
      if (q == 6) { WAD(i + 1, 3) WRL(i + 1, 2) }                                \
      if (q == 7) { WRH(i + 1, 2) }                                              \
      if (q == 8) { WRL(i + 1, 3) WRH(i + 1, 3) }                                \
    }                                                                            \
    POST                                                                         \
  }
      if (G2P != 2) __builtin_amdgcn_s_setprio(G2P);
      G2S(0, 0, OLD(3, 0) OW(0, 0, 12), ) G2S(0, 1, , ) G2S(0, 2, , ) G2S(0, 3, , ) G2S(0, 4, OLD(3, 1) OW(0, 1, 12), ) G2S(0, 5, , ) G2S(0, 6, , ) G2S(0, 7, , ) G2S(0, 8, OLD(3, 2) OW(0, 2, 12), ) G2S(0, 9, , ) G2S(0, 10, , ) G2S(0, 11, , ) G2S(0, 12, OLD(3, 3) OW(0, 3, 12), PICK(4) PADR(4)) G2S(0, 13, , ) G2S(0, 14, , ) G2S(0, 15, , )
      G2S(1, 0, OLD(4, 0) OW(1, 0, 12), ) G2S(1, 1, , ) G2S(1, 2, , ) G2S(1, 3, , ) G2S(1, 4, OLD(4, 1) OW(1, 1, 12), ) G2S(1, 5, , ) G2S(1, 6, , ) G2S(1, 7, , ) G2S(1, 8, OLD(4, 2) OW(1, 2, 12), ) G2S(1, 9, , ) G2S(1, 10, , ) G2S(1, 11, , ) G2S(1, 12, OLD(4, 3) OW(1, 3, 12), PICK(5) PADR(5)) G2S(1, 13, , ) G2S(1, 14, , ) G2S(1, 15, , )
      G2S(2, 0, OLD(5, 0) OW(2, 0, 12), ) G2S(2, 1, , ) G2S(2, 2, , ) G2S(2, 3, , ) G2S(2, 4, OLD(5, 1) OW(2, 1, 12), ) G2S(2, 5, , ) G2S(2, 6, , ) G2S(2, 7, , ) G2S(2, 8, OLD(5, 2) OW(2, 2, 12), ) G2S(2, 9, , ) G2S(2, 10, , ) G2S(2, 11, , ) G2S(2, 12, OLD(5, 3) OW(2, 3, 12), PICK(6) PADR(6)) G2S(2, 13, , ) G2S(2, 14, , ) G2S(2, 15, , )
      G2S(3, 0, OLD(6, 0) OW(3, 0, 12), ) G2S(3, 1, , ) G2S(3, 2, , ) G2S(3, 3, , ) G2S(3, 4, OLD(6, 1) OW(3, 1, 12), ) G2S(3, 5, , ) G2S(3, 6, , ) G2S(3, 7, , ) G2S(3, 8, OLD(6, 2) OW(3, 2, 12), ) G2S(3, 9, , ) G2S(3, 10, , ) G2S(3, 11, , ) G2S(3, 12, OLD(6, 3) OW(3, 3, 12), PICK(7) PADR(7)) G2S(3, 13, , ) G2S(3, 14, , ) G2S(3, 15, , )
      G2S(4, 0, OLD(7, 0) OW(4, 0, 12), ) G2S(4, 1, , ) G2S(4, 2, , ) G2S(4, 3, , ) G2S(4, 4, OLD(7, 1) OW(4, 1, 12), ) G2S(4, 5, , ) G2S(4, 6, , ) G2S(4, 7, , ) G2S(4, 8, OLD(7, 2) OW(4, 2, 12), ) G2S(4, 9, , ) G2S(4, 10, , ) G2S(4, 11, , ) G2S(4, 12, OLD(7, 3) OW(4, 3, 12), PICK(8) PADR(8)) G2S(4, 13, , ) G2S(4, 14, , ) G2S(4, 15, , )
      G2S(5, 0, OLD(8, 0) OW(5, 0, 12), ) G2S(5, 1, , ) G2S(5, 2, , ) G2S(5, 3, , ) G2S(5, 4, OLD(8, 1) OW(5, 1, 12), ) G2S(5, 5, , ) G2S(5, 6, , ) G2S(5, 7, , ) G2S(5, 8, OLD(8, 2) OW(5, 2, 12), ) G2S(5, 9, , ) G2S(5, 10, , ) G2S(5, 11, , ) G2S(5, 12, OLD(8, 3) OW(5, 3, 12), PICK(9) PADR(9)) G2S(5, 13, , ) G2S(5, 14, , ) G2S(5, 15, , )
      G2S(6, 0, OLD(9, 0) OW(6, 0, 12), ) G2S(6, 1, , ) G2S(6, 2, , ) G2S(6, 3, , ) G2S(6, 4, OLD(9, 1) OW(6, 1, 12), ) G2S(6, 5, , ) G2S(6, 6, , ) G2S(6, 7, , ) G2S(6, 8, OLD(9, 2) OW(6, 2, 12), ) G2S(6, 9, , ) G2S(6, 10, , ) G2S(6, 11, , ) G2S(6, 12, OLD(9, 3) OW(6, 3, 12), PICK(10) PADR(10)) G2S(6, 13, , ) G2S(6, 14, , ) G2S(6, 15, , )
      G2S(7, 0, OLD(10, 0) OW(7, 0, 12), ) G2S(7, 1, , ) G2S(7, 2, , ) G2S(7, 3, , ) G2S(7, 4, OLD(10, 1) OW(7, 1, 12), ) G2S(7, 5, , ) G2S(7, 6, , ) G2S(7, 7, , ) G2S(7, 8, OLD(10, 2) OW(7, 2, 12), ) G2S(7, 9, , ) G2S(7, 10, , ) G2S(7, 11, , ) G2S(7, 12, OLD(10, 3) OW(7, 3, 12), PICK(11) PADR(11)) G2S(7, 13, , ) G2S(7, 14, , ) G2S(7, 15, , )
      G2S(8, 0, OLD(11, 0) OW(8, 0, 12), ) G2S(8, 1, , ) G2S(8, 2, , ) G2S(8, 3, , ) G2S(8, 4, OLD(11, 1) OW(8, 1, 12), ) G2S(8, 5, , ) G2S(8, 6, , ) G2S(8, 7, , ) G2S(8, 8, OLD(11, 2) OW(8, 2, 12), ) G2S(8, 9, , ) G2S(8, 10, , ) G2S(8, 11, , ) G2S(8, 12, OLD(11, 3) OW(8, 3, 12), PICK(12) PADR(12)) G2S(8, 13, , ) G2S(8, 14, , ) G2S(8, 15, , )
      G2S(9, 0, OLD(12, 0) OW(9, 0, 12), ) G2S(9, 1, , ) G2S(9, 2, , ) G2S(9, 3, , ) G2S(9, 4, OLD(12, 1) OW(9, 1, 12), ) G2S(9, 5, , ) G2S(9, 6, , ) G2S(9, 7, , ) G2S(9, 8, OLD(12, 2) OW(9, 2, 12), ) G2S(9, 9, , ) G2S(9, 10, , ) G2S(9, 11, , ) G2S(9, 12, OLD(12, 3) OW(9, 3, 12), PICK(13) PADR(13)) G2S(9, 13, , ) G2S(9, 14, , ) G2S(9, 15, , )
      G2S(10, 0, OLD(13, 0) OW(10, 0, 12), ) G2S(10, 1, , ) G2S(10, 2, , ) G2S(10, 3, , ) G2S(10, 4, OLD(13, 1) OW(10, 1, 12), ) G2S(10, 5, , ) G2S(10, 6, , ) G2S(10, 7, , ) G2S(10, 8, OLD(13, 2) OW(10, 2, 12), ) G2S(10, 9, , ) G2S(10, 10, , ) G2S(10, 11, , ) G2S(10, 12, OLD(13, 3) OW(10, 3, 12), PICK(14) PADR(14)) G2S(10, 13, , ) G2S(10, 14, , ) G2S(10, 15, , )
      G2S(11, 0, OLD(14, 0) OW(11, 0, 12), ) G2S(11, 1, , ) G2S(11, 2, , ) G2S(11, 3, , ) G2S(11, 4, OLD(14, 1) OW(11, 1, 12), ) G2S(11, 5, , ) G2S(11, 6, , ) G2S(11, 7, , ) G2S(11, 8, OLD(14, 2) OW(11, 2, 12), ) G2S(11, 9, , ) G2S(11, 10, , ) G2S(11, 11, , ) G2S(11, 12, OLD(14, 3) OW(11, 3, 12), PICK(15) PADR(15)) G2S(11, 13, , ) G2S(11, 14, , ) G2S(11, 15, , )
      G2S(12, 0, OLD(15, 0) OW(12, 0, 12), ) G2S(12, 1, , ) G2S(12, 2, , ) G2S(12, 3, , ) G2S(12, 4, OLD(15, 1) OW(12, 1, 12), ) G2S(12, 5, , ) G2S(12, 6, , ) G2S(12, 7, , ) G2S(12, 8, OLD(15, 2) OW(12, 2, 12), ) G2S(12, 9, , ) G2S(12, 10, , ) G2S(12, 11, , ) G2S(12, 12, OLD(15, 3) OW(12, 3, 12), ) G2S(12, 13, , ) G2S(12, 14, , ) G2S(12, 15, , )
      G2S(13, 0, OW(13, 0, 11), ) G2S(13, 1, , ) G2S(13, 2, , ) G2S(13, 3, , ) G2S(13, 4, OW(13, 1, 10), ) G2S(13, 5, , ) G2S(13, 6, , ) G2S(13, 7, , ) G2S(13, 8, OW(13, 2, 9), ) G2S(13, 9, , ) G2S(13, 10, , ) G2S(13, 11, , ) G2S(13, 12, OW(13, 3, 8), ) G2S(13, 13, , ) G2S(13, 14, , ) G2S(13, 15, , )
      G2S(14, 0, OW(14, 0, 7), ) G2S(14, 1, , ) G2S(14, 2, , ) G2S(14, 3, , ) G2S(14, 4, OW(14, 1, 6), ) G2S(14, 5, , ) G2S(14, 6, , ) G2S(14, 7, , ) G2S(14, 8, OW(14, 2, 5), ) G2S(14, 9, , ) G2S(14, 10, , ) G2S(14, 11, , ) G2S(14, 12, OW(14, 3, 4), ) G2S(14, 13, , ) G2S(14, 14, , ) G2S(14, 15, , )
      G2S(15, 0, OW(15, 0, 3), ) G2S(15, 1, , ) G2S(15, 2, , ) G2S(15, 3, , ) G2S(15, 4, OW(15, 1, 2), ) G2S(15, 5, , ) G2S(15, 6, , ) G2S(15, 7, , ) G2S(15, 8, OW(15, 2, 1), ) G2S(15, 9, , ) G2S(15, 10, , ) G2S(15, 11, , ) G2S(15, 12, OW(15, 3, 0), ) G2S(15, 13, , ) G2S(15, 14, , ) G2S(15, 15, , )
      SB();
      if (G2P != 2) __builtin_amdgcn_s_setprio(2);
      const float chk = (a2[0][0][0] + a2[1][0][0]) + (a2[2][0][0] + a2[3][0][0]);
      if (tries || __builtin_amdgcn_ballot_w64(chk != chk) == 0ull) break;
      tries = 1;
      int spins = 0;
#pragma unroll 1
      for (int i = 0; i < 64; i++) {
        while (true) {
          u32x4 v;
          asm volatile("global_load_dwordx4 %0, %1, %2 sc1\n\ts_waitcnt vmcnt(0)" : "=v"(v) : "v"(l16), "s"(o_in + (i >> 2) * 8192 + (i & 3) * 1024) : "memory");
          if (__builtin_amdgcn_ballot_w64(max4(v) == SENT) == 0ull) break;
          if (++spins > SPIN_CAP) break;
        }
      }
    }
    PROBE(5)
    STAMP(7);
    {
    RELANE
    if (lane == 0) lflag[20 + wid] = 1u;
    lds_wait(lflag + 20 + (wid ^ 1));
    asm volatile("" ::: "memory");
    const int xw = kh * KH + wm * 8192, xr = (kh ^ 1) * KH + wm * 8192;
#pragma unroll
    for (int e = 0; e < 2; e++)
#pragma unroll
      for (int nt = 0; nt < 4; nt++) *(LAS f32x4*)(lds + xw + (e * 4 + nt) * 1024 + l16) = a2[2 + e][nt];
    asm volatile("" ::: "memory");
    if (lane == 0) lflag[24 + wid] = 1u;
    lds_wait(lflag + 24 + (wid ^ 2));
    asm volatile("" ::: "memory");
    if (bid == 0 && wid == 0 && lane == 0)
      asm volatile("global_store_dwordx2 %0, %1, %2 sc1" ::"v"(0u), "v"(TAGV), "s"(ws + WS_EPOCH) : "memory");
    const unsigned oo = (unsigned)((rb2 * 128 + wm * 64 + kh * 32 + i16) * 4096 + cb2 * 128 + (g & 1) * 32 + (g >> 1) * 16);
#pragma unroll
    for (int e = 0; e < 2; e++)
#pragma unroll
      for (int n2 = 0; n2 < 2; n2++) {
        unsigned r[2][2];
#pragma unroll
        for (int k = 0; k < 2; k++) {
          f32x4 o = *(const LAS f32x4*)(lds + xr + (e * 4 + 2 * n2 + k) * 1024 + l16);
          o += a2[e][2 * n2 + k];
          r[k][0] = pack2(o[0], o[1]); r[k][1] = pack2(o[2], o[3]);
        }
        auto a = __builtin_amdgcn_permlane16_swap(r[0][0], r[1][0], false, false);
        auto b = __builtin_amdgcn_permlane16_swap(r[0][1], r[1][1], false, false);
        u32x4 v = {a[0], b[0], a[1], b[1]};
        if (n2 == 0) st16_sc1<0, ONT>((char*)out + e * 65536, oo, v);
        else st16_sc1<64, ONT>((char*)out + e * 65536, oo, v);
      }
    }
  }
  PROBE(4)
  STAMP(8);
  asm volatile(".Lkend:");
}

extern "C" void kernel_launch(void* const* d_in, const int* in_sizes, int n_in, void* d_out, int out_size,
                              void* d_ws, size_t ws_size, hipStream_t stream) {
  (void)hipFuncSetAttribute((const void*)fused, hipFuncAttributeMaxDynamicSharedMemorySize, LDSZ);
  fused<<<256, 512, LDSZ, stream>>>((const u16*)d_in[0], (const u16*)d_in[1], (const u16*)d_in[2], (const u16*)d_in[3],
                                    (const u16*)d_in[4], (const u16*)d_in[5], (const u16*)d_in[6], (const u16*)d_in[7],
                                    (const u16*)d_in[8], (const u16*)d_in[9], (u16*)d_out, (char*)d_ws);
}
```
